```python
import torch

def _softmax(x, axis=-1):
    return torch.softmax(x, dim=axis)
def _gelu(x):
    return torch.nn.functional.gelu(x, approximate="tanh")
def _silu(x):
    return torch.nn.functional.silu(x)
def _sigmoid(x):
    return torch.sigmoid(x)
def _layernorm(x, axis=-1, eps=1e-5):
    m = x.mean(dim=axis, keepdim=True)
    v = x.var(dim=axis, keepdim=True, unbiased=False)
    return (x - m) / torch.sqrt(v + eps)
def _scatter_add(tgt, idx, src, axis=0):
    return tgt.clone().index_add_(axis, idx, src)
def _segment_sum(src, seg, n):
    out = torch.zeros((n,) + src.shape[1:], dtype=src.dtype)
    return out.index_add_(0, seg, src)
def _pad(x, pw):
    flat = tuple(v for pair in reversed(pw) for v in pair)
    return torch.nn.functional.pad(x, flat)

kernel_name = 'gated_mix__mha_block_cached__glu_gated_geglu__decode'

def setup_inputs():

    _w = (lambda a: (lambda t: t.to(torch.bfloat16) if t.is_floating_point() else t)(torch.as_tensor(a)))
    def _A_setup():
        B, Sq, D = 8, 1, 576
        H, Sk, Dh = 32, 4096, 128
        return {
            "x": _w((lambda *s: torch.randn(*s, dtype=torch.bfloat16) * 0.05)(B, Sq, D)),
            "k_cache": _w((lambda *s: torch.randn(*s, dtype=torch.bfloat16) * 3.2)(B, H, Sk, Dh)),
            "v_cache": _w((lambda *s: torch.randn(*s, dtype=torch.bfloat16) * 0.05)(B, H, Sk, Dh)),
            "wq": _w((lambda *s: torch.randn(*s, dtype=torch.bfloat16) * 3.2)(D, H * Dh) * (D ** -0.5)),
            "wo": _w((lambda *s: torch.randn(*s, dtype=torch.bfloat16) * 0.05)(H * Dh, D) * ((H * Dh) ** -0.5)),
        }
    _A_in = _A_setup()

    def _B_setup():
        B, S, Dm, Df = 8, 1, 576, 4608
        return {
            "x": _w((lambda *s: torch.randn(*s, dtype=torch.bfloat16) * 0.05)(B, S, Dm)),
            "w_gate": _w((lambda *s: torch.randn(*s, dtype=torch.bfloat16) * 0.2)(Dm, Df) * (Dm ** -0.5)),
            "w_up": _w((lambda *s: torch.randn(*s, dtype=torch.bfloat16) * 0.2)(Dm, Df) * (Dm ** -0.5)),
            "w_down": _w((lambda *s: torch.randn(*s, dtype=torch.bfloat16) * 0.2)(Df, Dm) * (Df ** -0.5)),
        }
    _B_in = _B_setup()

    return {
        "A_x": _A_in["x"],
        "A_k_cache": _A_in["k_cache"],
        "A_v_cache": _A_in["v_cache"],
        "A_wq": _A_in["wq"],
        "A_wo": _A_in["wo"],
        "B_x": _B_in["x"],
        "B_w_gate": _B_in["w_gate"],
        "B_w_up": _B_in["w_up"],
        "B_w_down": _B_in["w_down"],
        "gate_w": _w((lambda *s: torch.randn(*s, dtype=torch.bfloat16) * 0.05)(576) * 16.0),
    }

def reference(A_x, A_k_cache, A_v_cache, A_wq, A_wo, B_x, B_w_gate, B_w_up, B_w_down, gate_w):
    def _A_ref(x, k_cache, v_cache, wq, wo):
        B, Sq, D = x.shape
        H, Dh = 32, 128
        q = (lambda x, *d: x.permute(*d))((x @ wq).reshape(B, Sq, H, Dh), 0, 2, 1, 3)
        scores = torch.einsum("bhqd,bhkd->bhqk", q, k_cache) * (Dh ** -0.5)
        p = _softmax(scores, axis=-1)
        o = torch.einsum("bhqk,bhkd->bhqd", p, v_cache)
        return (lambda x, *d: x.permute(*d))(o, 0, 2, 1, 3).reshape(B, Sq, H * Dh) @ wo

    def _B_ref(x, w_gate, w_up, w_down):
        g = torch.einsum("bsd,df->bsf", x, w_gate)
        u = torch.einsum("bsd,df->bsf", x, w_up)
        h = _gelu(g) * u
        return torch.einsum("bsf,fd->bsd", h, w_down)

    x = A_x
    g = _sigmoid((x * gate_w).sum(-1, keepdims=True))
    a = _A_ref(x, A_k_cache, A_v_cache, A_wq, A_wo)
    b = _B_ref(x, B_w_gate, B_w_up, B_w_down)
    return g * a + (1 - g) * b
```

```cpp
#include <hip/hip_runtime.h>
#include <hip/hip_bf16.h>
#include <stdint.h>

typedef unsigned v4u __attribute__((ext_vector_type(4)));
typedef unsigned v2u __attribute__((ext_vector_type(2)));
typedef float v4f __attribute__((ext_vector_type(4)));
typedef float v2f __attribute__((ext_vector_type(2)));
typedef __bf16 bf16x2 __attribute__((ext_vector_type(2)));
typedef unsigned long long u64;

#define RLX_AGENT __ATOMIC_RELAXED, __HIP_MEMORY_SCOPE_AGENT

constexpr int NB = 8, NH = 32, SK = 4096, DH = 128, DM = 576, DF = 4608;
constexpr int RING = 16;
constexpr int TILES = 256;
constexpr size_t WS_EPOCH = 0;
constexpr size_t WS_INBOX = 4096;
constexpr unsigned SPIN_CAP = 1u << 18;

constexpr int L_SC = 0;
constexpr int L_QP = L_SC + 4096;
constexpr int L_GU = L_QP + 512;
constexpr int L_H = L_GU + 12 * 288;
constexpr int L_MRG = L_H + 72;
constexpr int L_OP = L_MRG + 4 * 132;
constexpr int L_RED = L_OP + 72;
constexpr int L_FIN = L_RED + 3 * 576;
constexpr int L_MISC = L_FIN + 576;
constexpr int L_TOTAL = L_MISC + 8;

__device__ __forceinline__ float dot2(unsigned a, unsigned b, float acc) {
  return __builtin_amdgcn_fdot2_f32_bf16(__builtin_bit_cast(bf16x2, a), __builtin_bit_cast(bf16x2, b), acc, false);
}
__device__ __forceinline__ unsigned cvtpk(float lo, float hi) {
  unsigned r; asm("v_cvt_pk_bf16_f32 %0, %1, %2" : "=v"(r) : "v"(lo), "v"(hi)); return r;
}
__device__ __forceinline__ float bf_round(float x) { return __builtin_bit_cast(float, cvtpk(x, x) << 16); }
template <int CTRL> __device__ __forceinline__ float dpp(float x) {
  return __builtin_bit_cast(float, __builtin_amdgcn_mov_dpp(__builtin_bit_cast(int, x), CTRL, 0xf, 0xf, true));
}
constexpr int XOR1 = 0xB1, XOR2 = 0x4E, XOR7 = 0x141, XOR8 = 0x128, BCAST = 0x150;
__device__ __forceinline__ float xrow_sum(float x) {
  auto s = __builtin_amdgcn_permlane16_swap(__float_as_uint(x), __float_as_uint(x), false, false);
  x = __uint_as_float(s[0]) + __uint_as_float(s[1]);
  auto t = __builtin_amdgcn_permlane32_swap(__float_as_uint(x), __float_as_uint(x), false, false);
  return __uint_as_float(t[0]) + __uint_as_float(t[1]);
}
__device__ __forceinline__ float xrow_max(float x) {
  auto s = __builtin_amdgcn_permlane16_swap(__float_as_uint(x), __float_as_uint(x), false, false);
  x = fmaxf(__uint_as_float(s[0]), __uint_as_float(s[1]));
  auto t = __builtin_amdgcn_permlane32_swap(__float_as_uint(x), __float_as_uint(x), false, false);
  return fmaxf(__uint_as_float(t[0]), __uint_as_float(t[1]));
}
__device__ __forceinline__ float wave_sum(float x) {
  x += dpp<XOR1>(x); x += dpp<XOR2>(x); x += dpp<XOR7>(x); x += dpp<XOR8>(x);
  return xrow_sum(x);
}
__device__ __forceinline__ float wave_max(float x) {
  x = fmaxf(x, dpp<XOR1>(x)); x = fmaxf(x, dpp<XOR2>(x)); x = fmaxf(x, dpp<XOR7>(x)); x = fmaxf(x, dpp<XOR8>(x));
  return xrow_max(x);
}
__device__ __forceinline__ v4u ld_nt(const char* p) { return __builtin_nontemporal_load(reinterpret_cast<const v4u*>(p)); }
__device__ __forceinline__ v4u ld_w(const char* p) { return *reinterpret_cast<const v4u*>(p); }
__device__ __forceinline__ void wg_barrier() {
  asm volatile("s_waitcnt lgkmcnt(0)" ::: "memory");
  __builtin_amdgcn_s_barrier();
  asm volatile("" ::: "memory");
}
#define SB() __builtin_amdgcn_sched_barrier(0)

__device__ __forceinline__ void gemv2(float (&acc)[8], v4u w0, v4u w1, unsigned xp) {
#pragma unroll
  for (int t = 0; t < 4; ++t) {
    acc[2 * t] = dot2(__builtin_amdgcn_perm(w1[t], w0[t], 0x05040100u), xp, acc[2 * t]);
    acc[2 * t + 1] = dot2(__builtin_amdgcn_perm(w1[t], w0[t], 0x07060302u), xp, acc[2 * t + 1]);
  }
}

typedef __attribute__((address_space(3))) unsigned lds_u32;
__device__ __forceinline__ void aux_barrier(lds_u32* cnt, unsigned target, int lane) {
  asm volatile("s_waitcnt lgkmcnt(0)" ::: "memory");
  if (lane == 0) __hip_atomic_fetch_add(cnt, 1u, __ATOMIC_RELAXED, __HIP_MEMORY_SCOPE_WORKGROUP);
#pragma unroll 1
  for (unsigned s = 0; s < SPIN_CAP; ++s) {
    const unsigned v = __builtin_amdgcn_readfirstlane(__hip_atomic_load(cnt, __ATOMIC_RELAXED, __HIP_MEMORY_SCOPE_WORKGROUP));
    if (v >= target) break;
    __builtin_amdgcn_s_sleep(2);
  }
  asm volatile("" ::: "memory");
}

__global__ void __launch_bounds__(512, 2)
fused_decode(const char* __restrict__ xg, const char* kc, const char* vc,
             const char* __restrict__ wq, const char* __restrict__ wo, const char* __restrict__ wg,
             const char* __restrict__ wu, const char* __restrict__ wd, const char* __restrict__ gw,
             unsigned* __restrict__ out, char* __restrict__ ws) {
  __shared__ __attribute__((aligned(16))) float lds[L_TOTAL];
  unsigned* ldsu = reinterpret_cast<unsigned*>(lds);
  const int tid = threadIdx.x, lane = tid & 63;
  const int wave = __builtin_amdgcn_readfirstlane(tid >> 6);
  const int bh = blockIdx.x, b = bh >> 5, h = bh & 31;
  const int r = lane >> 4, c = lane & 15;

  if (wave < 4) {
    const char* kb = kc + (size_t)bh * (SK * DH * 2) + wave * 1024;
    const char* vb = vc + (size_t)bh * (SK * DH * 2) + wave * 1024;
    const unsigned voff = lane * 16;
    v4u a[RING];
#pragma unroll
    for (int j = 0; j < RING; ++j) { a[j] = ld_nt(kb + j * 4096 + voff); asm volatile("" ::: "memory"); SB(); }
    wg_barrier();
    unsigned q[4];
    {
      v4f lo = {0.f, 0.f, 0.f, 0.f}, hi = {0.f, 0.f, 0.f, 0.f};
#pragma unroll
      for (int w = 0; w < 4; ++w) {
        lo += *reinterpret_cast<const v4f*>(lds + L_QP + w * 128 + 8 * c);
        hi += *reinterpret_cast<const v4f*>(lds + L_QP + w * 128 + 8 * c + 4);
      }
      q[0] = cvtpk(lo[0], lo[1]); q[1] = cvtpk(lo[2], lo[3]); q[2] = cvtpk(hi[0], hi[1]); q[3] = cvtpk(hi[2], hi[3]);
    }
    constexpr float C2 = 0.08838834764831845f * 1.4426950408889634f;
    float* sc = lds + L_SC + wave * 1024 + lane;
    float m = -INFINITY;
    for (int it = 0; it < TILES / RING; ++it) {
      const char* pf = (it + 1 < TILES / RING) ? kb + (size_t)(it + 1) * (RING * 4096) : vb;
#pragma unroll
      for (int gg = 0; gg < RING / 16; ++gg) {
        float d[16];
#pragma unroll
        for (int j = 0; j < 16; ++j) {
          const v4u t = a[gg * 16 + j];
          float s = dot2(t[0], q[0], 0.f);
          s = dot2(t[1], q[1], s); s = dot2(t[2], q[2], s); s = dot2(t[3], q[3], s);
          asm volatile("" : "+v"(s)::"memory");
          d[j] = s;
          SB();
          a[gg * 16 + j] = ld_nt(pf + (gg * 16 + j) * 4096 + voff);
          asm volatile("" ::: "memory");
          SB();
        }
#define TR_STEP(HALF, CTRL, BIT)                                                                           \
  _Pragma("unroll") for (int u = 0; u < (HALF); ++u) {                                                      \
    const float lo_ = d[u], hi_ = d[u + (HALF)];                                                            \
    const float t1_ = lo_ + dpp<CTRL>(lo_), t2_ = hi_ + dpp<CTRL>(hi_);                                     \
    d[u] = (c & (BIT)) ? t2_ : t1_;                                                                         \
  }
        TR_STEP(8, XOR8, 8) TR_STEP(4, XOR7, 4) TR_STEP(2, XOR2, 2) TR_STEP(1, XOR1, 1)
#undef TR_STEP
        const float s = d[0] * C2;
        m = fmaxf(m, s);
        sc[(it * (RING / 16) + gg) * 64] = s;
      }
    }
    m = wave_max(m);
    float acc[8] = {0.f, 0.f, 0.f, 0.f, 0.f, 0.f, 0.f, 0.f};
    float l = 0.f;
#define PV_GROUP(GIDX, SLOT0, RELOAD, PFB)                                                                  \
  {                                                                                                         \
    const float p = __builtin_amdgcn_exp2f(sc[(GIDX) * 64] - m);                                            \
    l += p;                                                                                                 \
    float pj;                                                                                               \
    PV_TILE(0, SLOT0, RELOAD, PFB) PV_TILE(1, SLOT0, RELOAD, PFB) PV_TILE(2, SLOT0, RELOAD, PFB)            \
    PV_TILE(3, SLOT0, RELOAD, PFB) PV_TILE(4, SLOT0, RELOAD, PFB) PV_TILE(5, SLOT0, RELOAD, PFB)            \
    PV_TILE(6, SLOT0, RELOAD, PFB) PV_TILE(7, SLOT0, RELOAD, PFB) PV_TILE(8, SLOT0, RELOAD, PFB)            \
    PV_TILE(9, SLOT0, RELOAD, PFB) PV_TILE(10, SLOT0, RELOAD, PFB) PV_TILE(11, SLOT0, RELOAD, PFB)          \
    PV_TILE(12, SLOT0, RELOAD, PFB) PV_TILE(13, SLOT0, RELOAD, PFB) PV_TILE(14, SLOT0, RELOAD, PFB)         \
    PV_TILE(15, SLOT0, RELOAD, PFB)                                                                         \
  }
#define PV_TILE(J, SLOT0, RELOAD, PFB)                                                                      \
  {                                                                                                         \
    const v4u t = a[(SLOT0) + (J)];                                                                         \
    pj = dpp<BCAST + (J)>(p);                                                                               \
    _Pragma("unroll") for (int k = 0; k < 4; ++k) {                                                         \
      acc[2 * k] = fmaf(pj, __builtin_bit_cast(float, t[k] << 16), acc[2 * k]);                             \
      acc[2 * k + 1] = fmaf(pj, __builtin_bit_cast(float, t[k] & 0xffff0000u), acc[2 * k + 1]);             \
    }                                                                                                       \
    asm volatile("" : "+v"(acc[0]), "+v"(acc[1]), "+v"(acc[2]), "+v"(acc[3]), "+v"(acc[4]), "+v"(acc[5]),   \
                 "+v"(acc[6]), "+v"(acc[7])::"memory");                                                     \
    SB();                                                                                                   \
    if (RELOAD) a[(SLOT0) + (J)] = ld_nt((PFB) + ((SLOT0) + (J)) * 4096 + voff);                            \
    asm volatile("" ::: "memory");                                                                          \
    SB();                                                                                                   \
  }
    for (int it = 0; it < TILES / RING - 1; ++it) {
      const char* pf = vb + (size_t)(it + 1) * (RING * 4096);
#pragma unroll
      for (int gg = 0; gg < RING / 16; ++gg) PV_GROUP(it * (RING / 16) + gg, gg * 16, true, pf)
    }
#pragma unroll
    for (int gg = 0; gg < RING / 16; ++gg) PV_GROUP((TILES / RING - 1) * (RING / 16) + gg, gg * 16, false, vb)
#undef PV_GROUP
#undef PV_TILE
    l = wave_sum(l);
#pragma unroll
    for (int k = 0; k < 8; ++k) acc[k] = xrow_sum(acc[k]);
    float* rec = lds + L_MRG + wave * 132;
    if (r == 0) {
      *reinterpret_cast<v4f*>(rec + 4 + 8 * c) = v4f{acc[0], acc[1], acc[2], acc[3]};
      *reinterpret_cast<v4f*>(rec + 4 + 8 * c + 4) = v4f{acc[4], acc[5], acc[6], acc[7]};
      if (c == 0) { rec[0] = m; rec[1] = l; }
    }
    wg_barrier();
    if (tid < 128) {
      const float* mr = lds + L_MRG;
      const float M = fmaxf(fmaxf(mr[0], mr[132]), fmaxf(mr[264], mr[396]));
      float L = 0.f, o = 0.f;
#pragma unroll
      for (int w = 0; w < 4; ++w) {
        const float e = __builtin_amdgcn_exp2f(mr[w * 132] - M);
        L = fmaf(e, mr[w * 132 + 1], L);
        o = fmaf(e, mr[w * 132 + 4 + tid], o);
      }
      o = o / L;
      const int rg = tid / 44, idx = tid - rg * 44;
      reinterpret_cast<unsigned short*>(ldsu + L_OP)[rg * 48 + idx] = (unsigned short)(cvtpk(o, o) & 0xffffu);
    }
    wg_barrier();
    wg_barrier();
  } else {
    const int aw = wave - 4, ta = tid - 256;
    const int fs = (h & 7) * 4 + (h >> 3);
    const char* xb = xg + b * (DM * 2);
    {
      const int k0 = 144 * aw + 36 * r;
      const char* wp = wq + ((size_t)(144 * aw) * (NH * DH) + h * DH) * 2;
      const unsigned vo = (unsigned)(36 * r * (NH * DH) + 8 * c) * 2u;
      v4u wv[36];
#pragma unroll
      for (int i = 0; i < 36; ++i) wv[i] = ld_w(wp + (size_t)i * (NH * DH * 2) + vo);
      v2u xv[9];
#pragma unroll
      for (int i = 0; i < 9; ++i) xv[i] = *reinterpret_cast<const v2u*>(xb + k0 * 2 + i * 8);
      u64 ep = 0;
      if (ta == 0) ep = __hip_atomic_load(reinterpret_cast<u64*>(ws + WS_EPOCH + b * 256), RLX_AGENT);
      SB();
      float acc[8] = {0.f, 0.f, 0.f, 0.f, 0.f, 0.f, 0.f, 0.f};
#pragma unroll
      for (int i = 0; i < 18; ++i) gemv2(acc, wv[2 * i], wv[2 * i + 1], xv[i >> 1][i & 1]);
#pragma unroll
      for (int k = 0; k < 8; ++k) acc[k] = xrow_sum(acc[k]);
      if (r == 0) {
        *reinterpret_cast<v4f*>(lds + L_QP + aw * 128 + 8 * c) = v4f{acc[0], acc[1], acc[2], acc[3]};
        *reinterpret_cast<v4f*>(lds + L_QP + aw * 128 + 8 * c + 4) = v4f{acc[4], acc[5], acc[6], acc[7]};
      }
      if (ta == 0) {
        ldsu[L_MISC + 0] = 0u;
        ldsu[L_MISC + 2] = (unsigned)ep; ldsu[L_MISC + 3] = (unsigned)(ep >> 32);
      }
      if (ta < 4) ldsu[L_OP + 2 * 24 + 20 + ta] = 0u;
    }
    wg_barrier();
    lds_u32* cnt = (lds_u32*)(ldsu + L_MISC);
    {
      const int r3 = min(lane / 18, 2), c18 = lane % 18;
      const int k0 = 144 * aw + 48 * r3;
      const size_t woff = ((size_t)(144 * aw) * DF + fs * 144) * 2;
      const unsigned vo = (unsigned)(48 * r3 * DF + 8 * c18) * 2u;
      v4u xv[6];
#pragma unroll
      for (int i = 0; i < 6; ++i) xv[i] = *reinterpret_cast<const v4u*>(xb + k0 * 2 + i * 16);
      float ag[8] = {0.f, 0.f, 0.f, 0.f, 0.f, 0.f, 0.f, 0.f}, au[8] = {0.f, 0.f, 0.f, 0.f, 0.f, 0.f, 0.f, 0.f};
#pragma unroll
      for (int bt = 0; bt < 3; ++bt) {
        v4u g[16], u[16];
#pragma unroll
        for (int i = 0; i < 16; ++i) {
          g[i] = ld_w(wg + woff + (size_t)(bt * 16 + i) * (DF * 2) + vo);
          u[i] = ld_w(wu + woff + (size_t)(bt * 16 + i) * (DF * 2) + vo);
        }
        SB();
#pragma unroll
        for (int i = 0; i < 8; ++i) {
          const int pi = bt * 8 + i;
          gemv2(ag, g[2 * i], g[2 * i + 1], xv[pi >> 2][pi & 3]);
          gemv2(au, u[2 * i], u[2 * i + 1], xv[pi >> 2][pi & 3]);
        }
        SB();
      }
      {
        float* dst = lds + L_GU + (aw * 3 + r3) * 288 + 8 * c18;
        *reinterpret_cast<v4f*>(dst) = v4f{ag[0], ag[1], ag[2], ag[3]};
        *reinterpret_cast<v4f*>(dst + 4) = v4f{ag[4], ag[5], ag[6], ag[7]};
        *reinterpret_cast<v4f*>(dst + 144) = v4f{au[0], au[1], au[2], au[3]};
        *reinterpret_cast<v4f*>(dst + 148) = v4f{au[4], au[5], au[6], au[7]};
      }
    }
    aux_barrier(cnt, 4, lane);
    if (ta < 144) {
      float g = 0.f, u = 0.f;
#pragma unroll
      for (int p = 0; p < 12; ++p) { g += lds[L_GU + p * 288 + ta]; u += lds[L_GU + p * 288 + 144 + ta]; }
      g = bf_round(g); u = bf_round(u);
      const float inner = 0.7978845608028654f * (g + 0.044715f * g * g * g);
      const float ge = bf_round(0.5f * g * (1.f + tanhf(inner)));
      const float hh = ge * u;
      reinterpret_cast<unsigned short*>(ldsu + L_H)[ta] = (unsigned short)(cvtpk(hh, hh) & 0xffffu);
    }
    aux_barrier(cnt, 8, lane);
    const int rg = min(ta / 72, 2), cg = ta % 72;
    float accd[8] = {0.f, 0.f, 0.f, 0.f, 0.f, 0.f, 0.f, 0.f};
    {
      const char* wp = wd + (size_t)(fs * 144) * DM * 2;
      const unsigned vo = (unsigned)(48 * rg * DM + 8 * cg) * 2u;
      v4u hv[6];
#pragma unroll
      for (int i = 0; i < 6; ++i) hv[i] = *reinterpret_cast<const v4u*>(ldsu + L_H + 24 * rg + 4 * i);
#pragma unroll
      for (int bt = 0; bt < 2; ++bt) {
        v4u w[24];
#pragma unroll
        for (int i = 0; i < 24; ++i) w[i] = ld_w(wp + (size_t)(bt * 24 + i) * (DM * 2) + vo);
        SB();
#pragma unroll
        for (int i = 0; i < 12; ++i) {
          const int pi = bt * 12 + i;
          gemv2(accd, w[2 * i], w[2 * i + 1], hv[pi >> 2][pi & 3]);
        }
        SB();
      }
    }
    float gate;
    {
      float z = 0.f;
#pragma unroll
      for (int i = 0; i < 5; ++i) {
        const int dw = lane + 64 * i;
        if (dw < 288) z = dot2(reinterpret_cast<const unsigned*>(xb)[dw], reinterpret_cast<const unsigned*>(gw)[dw], z);
      }
      z = wave_sum(z);
      gate = 1.f / (1.f + __expf(-z));
    }
#pragma unroll
    for (int k = 0; k < 8; ++k) accd[k] *= (1.f - gate);
    v4u wv[44];
    {
      const char* wp = wo + (size_t)(h * DH) * DM * 2;
      const unsigned vo = (unsigned)(44 * rg * DM + 8 * cg) * 2u;
      const unsigned vo2 = rg == 2 ? vo - 4u * (DM * 2) : vo;
#pragma unroll
      for (int i = 0; i < 44; ++i) wv[i] = ld_w(wp + (size_t)i * (DM * 2) + (i < 40 ? vo : vo2));
      SB();
#pragma unroll
      for (int i = 0; i < 22; ++i) {
#pragma unroll
        for (int t = 0; t < 4; ++t) {
          const unsigned w0 = wv[2 * i][t], w1 = wv[2 * i + 1][t];
          wv[2 * i][t] = __builtin_amdgcn_perm(w1, w0, 0x05040100u);
          wv[2 * i + 1][t] = __builtin_amdgcn_perm(w1, w0, 0x07060302u);
        }
      }
      SB();
    }
    wg_barrier();
    wg_barrier();
    {
      v4u ov[6];
#pragma unroll
      for (int i = 0; i < 6; ++i) ov[i] = *reinterpret_cast<const v4u*>(ldsu + L_OP + 24 * rg + 4 * i);
      float acco[8] = {0.f, 0.f, 0.f, 0.f, 0.f, 0.f, 0.f, 0.f};
#pragma unroll
      for (int i = 0; i < 22; ++i) {
        const unsigned op = ov[i >> 2][i & 3];
#pragma unroll
        for (int t = 0; t < 4; ++t) {
          acco[2 * t] = dot2(wv[2 * i][t], op, acco[2 * t]);
          acco[2 * t + 1] = dot2(wv[2 * i + 1][t], op, acco[2 * t + 1]);
        }
      }
      {
        float* dst = lds + L_RED + rg * 576 + 8 * cg;
        *reinterpret_cast<v4f*>(dst) = v4f{fmaf(gate, acco[0], accd[0]), fmaf(gate, acco[1], accd[1]), fmaf(gate, acco[2], accd[2]), fmaf(gate, acco[3], accd[3])};
        *reinterpret_cast<v4f*>(dst + 4) = v4f{fmaf(gate, acco[4], accd[4]), fmaf(gate, acco[5], accd[5]), fmaf(gate, acco[6], accd[6]), fmaf(gate, acco[7], accd[7])};
      }
    }
    wg_barrier();
  }

  const u64 ep1 = (((u64)ldsu[L_MISC + 3] << 32) | ldsu[L_MISC + 2]) + 1ull;
  u64 tag;
  {
    u64 z = ep1 * 0x9E3779B97F4A7C15ull;
    z = (z ^ (z >> 30)) * 0xBF58476D1CE4E5B9ull;
    z = (z ^ (z >> 27)) * 0x94D049BB133111EBull;
    z ^= z >> 31;
    tag = (z & 0xFFFFFFFF00000000ull) | ((ep1 & 0xFFull) << 24);
  }
  u64* inbox = reinterpret_cast<u64*>(ws + WS_INBOX) + (size_t)b * (NH * NH * 18);
  if (tid < 288) {
    const v2f p0 = *reinterpret_cast<const v2f*>(lds + L_RED + 2 * tid);
    const v2f p1 = *reinterpret_cast<const v2f*>(lds + L_RED + 576 + 2 * tid);
    const v2f p2 = *reinterpret_cast<const v2f*>(lds + L_RED + 1152 + 2 * tid);
    const float v0 = p0[0] + p1[0] + p2[0], v1 = p0[1] + p1[1] + p2[1];
    const int hc = tid / 9, j = tid - hc * 9;
    u64* dst = inbox + (size_t)(hc * NH + h) * 18 + 2 * j;
    __hip_atomic_store(dst, tag | (u64)(__float_as_uint(v0) >> 8), RLX_AGENT);
    __hip_atomic_store(dst + 1, tag | (u64)(__float_as_uint(v1) >> 8), RLX_AGENT);
    u64* src = inbox + (size_t)h * (NH * 18) + 2 * tid;
    u64 g0 = 0, g1 = 0;
#pragma unroll 1
    for (unsigned s = 0; s < SPIN_CAP; ++s) {
      g0 = __hip_atomic_load(src, RLX_AGENT);
      g1 = __hip_atomic_load(src + 1, RLX_AGENT);
      if (((g0 ^ tag) >> 24) == 0 && ((g1 ^ tag) >> 24) == 0) break;
      __builtin_amdgcn_s_sleep(1);
    }
    *reinterpret_cast<v2f*>(lds + L_FIN + 2 * tid) =
        v2f{__uint_as_float((unsigned)g0 << 8), __uint_as_float((unsigned)g1 << 8)};
  }
  wg_barrier();
  if (tid < 9) {
    float s0 = 0.f, s1 = 0.f;
#pragma unroll
    for (int p = 0; p < 32; ++p) {
      const v2f v = *reinterpret_cast<const v2f*>(lds + L_FIN + p * 18 + 2 * tid);
      s0 += v[0]; s1 += v[1];
    }
    out[b * (DM / 2) + h * 9 + tid] = cvtpk(s0, s1);
    if (tid == 0 && h == 0) __hip_atomic_store(reinterpret_cast<u64*>(ws + WS_EPOCH + b * 256), ep1, RLX_AGENT);
  }
}

extern "C" void kernel_launch(void* const* d_in, const int* in_sizes, int n_in, void* d_out, int out_size, void* d_ws,
                              size_t ws_size, hipStream_t stream) {
  (void)in_sizes; (void)n_in; (void)out_size;
  if (ws_size < WS_INBOX + (size_t)NB * NH * NH * 18 * 8) return;
  fused_decode<<<dim3(NB * NH), dim3(512), 0, stream>>>(
      (const char*)d_in[0], (const char*)d_in[1], (const char*)d_in[2], (const char*)d_in[3], (const char*)d_in[4],
      (const char*)d_in[6], (const char*)d_in[7], (const char*)d_in[8], (const char*)d_in[9], (unsigned*)d_out,
      (char*)d_ws);
}
```

```cpp
#include <hip/hip_runtime.h>
#include <hip/hip_bf16.h>
#include <stdint.h>

typedef unsigned v4u __attribute__((ext_vector_type(4)));
typedef unsigned v2u __attribute__((ext_vector_type(2)));
typedef float v4f __attribute__((ext_vector_type(4)));
typedef float v2f __attribute__((ext_vector_type(2)));
typedef __bf16 bf16x2 __attribute__((ext_vector_type(2)));
typedef unsigned long long u64;

#define RLX_AGENT __ATOMIC_RELAXED, __HIP_MEMORY_SCOPE_AGENT

constexpr int NB = 8, NH = 32, SK = 4096, DH = 128, DM = 576, DF = 4608;
#define RINGD 16
constexpr int RING = RINGD;
constexpr int TILES = 256;
constexpr size_t WS_EPOCH = 0;
constexpr size_t WS_INBOX = 4096;
constexpr int WS_GU = 4096 + 8 * 32 * 32 * 18 * 8;
constexpr int WS_QIN = WS_GU + 2 * 8 * 2304 * 8;
constexpr int WS_TOTAL = WS_QIN + 256 * 4096;
constexpr unsigned SPIN_CAP = 1u << 18;

constexpr int L_SC = 0;
constexpr int L_QP = L_SC + 4096;
constexpr int L_GU = L_QP + 512;
constexpr int L_H = L_GU + 4096;
constexpr int L_MRG = L_H + 72;
constexpr int L_OP = L_MRG + 4 * 132;
constexpr int L_RED = L_OP + 72;
constexpr int L_FIN = L_RED + 3 * 576;
constexpr int L_MISC = L_FIN + 576;
constexpr int L_TAG = L_MISC + 8;
constexpr int L_TOTAL = L_TAG + 8;

__device__ __forceinline__ float dot2(unsigned a, unsigned b, float acc) {
  return __builtin_amdgcn_fdot2_f32_bf16(__builtin_bit_cast(bf16x2, a), __builtin_bit_cast(bf16x2, b), acc, false);
}
__device__ __forceinline__ unsigned cvtpk(float lo, float hi) {
  unsigned r; asm("v_cvt_pk_bf16_f32 %0, %1, %2" : "=v"(r) : "v"(lo), "v"(hi)); return r;
}
__device__ __forceinline__ float bf_round(float x) { return __builtin_bit_cast(float, cvtpk(x, x) << 16); }
template <int CTRL> __device__ __forceinline__ float dpp(float x) {
  return __builtin_bit_cast(float, __builtin_amdgcn_mov_dpp(__builtin_bit_cast(int, x), CTRL, 0xf, 0xf, true));
}
constexpr int XOR1 = 0xB1, XOR2 = 0x4E, XOR7 = 0x141, XOR8 = 0x128, BCAST = 0x150;
__device__ __forceinline__ float xrow_sum(float x) {
  auto s = __builtin_amdgcn_permlane16_swap(__float_as_uint(x), __float_as_uint(x), false, false);
  x = __uint_as_float(s[0]) + __uint_as_float(s[1]);
  auto t = __builtin_amdgcn_permlane32_swap(__float_as_uint(x), __float_as_uint(x), false, false);
  return __uint_as_float(t[0]) + __uint_as_float(t[1]);
}
__device__ __forceinline__ float xrow_max(float x) {
  auto s = __builtin_amdgcn_permlane16_swap(__float_as_uint(x), __float_as_uint(x), false, false);
  x = fmaxf(__uint_as_float(s[0]), __uint_as_float(s[1]));
  auto t = __builtin_amdgcn_permlane32_swap(__float_as_uint(x), __float_as_uint(x), false, false);
  return fmaxf(__uint_as_float(t[0]), __uint_as_float(t[1]));
}
__device__ __forceinline__ float wave_sum(float x) {
  x += dpp<XOR1>(x); x += dpp<XOR2>(x); x += dpp<XOR7>(x); x += dpp<XOR8>(x);
  return xrow_sum(x);
}
__device__ __forceinline__ float wave_max(float x) {
  x = fmaxf(x, dpp<XOR1>(x)); x = fmaxf(x, dpp<XOR2>(x)); x = fmaxf(x, dpp<XOR7>(x)); x = fmaxf(x, dpp<XOR8>(x));
  return xrow_max(x);
}
__device__ __forceinline__ v4u ld_nt(const char* p) { return __builtin_nontemporal_load(reinterpret_cast<const v4u*>(p)); }
__device__ __forceinline__ v4u ld_w(const char* p) { return *reinterpret_cast<const v4u*>(p); }
__device__ __forceinline__ void wg_barrier() {
  asm volatile("s_waitcnt lgkmcnt(0)" ::: "memory");
  __builtin_amdgcn_s_barrier();
  asm volatile("" ::: "memory");
}
#define SB() __builtin_amdgcn_sched_barrier(0)
#define PK 5
#define PV PK
#define PA 2
#define PAG PA
#define P1 5
#define PARK 2
#define SWAP 256
#define QS0 32
#define QS1 8
#define GATE_IT 5
#define LDG ld_nt
#define LDD ld_w
#define LDO ld_w
#define LDQ ld_nt
constexpr int PACE_K = PK, PACE_V = PV, PACE_A = PA;
#define PWAIT(N) asm volatile("s_waitcnt vmcnt(%0)" ::"n"((N) - 1) : "memory")
#define AWAIT(I) do { SB(); if ((I) >= PACE_A) PWAIT(PACE_A); SB(); } while (0)

__device__ __forceinline__ void gemv2(float (&acc)[8], v4u w0, v4u w1, unsigned xp) {
#pragma unroll
  for (int t = 0; t < 4; ++t) {
    acc[2 * t] = dot2(__builtin_amdgcn_perm(w1[t], w0[t], 0x05040100u), xp, acc[2 * t]);
    acc[2 * t + 1] = dot2(__builtin_amdgcn_perm(w1[t], w0[t], 0x07060302u), xp, acc[2 * t + 1]);
  }
}

typedef __bf16 bf16x8 __attribute__((ext_vector_type(8)));
__device__ __forceinline__ v4f mfma32(v4u a, v4u b, v4f c) {
  return __builtin_amdgcn_mfma_f32_16x16x32_bf16(__builtin_bit_cast(bf16x8, a), __builtin_bit_cast(bf16x8, b), c, 0, 0, 0);
}
__device__ __forceinline__ unsigned pair(unsigned row1, unsigned row0, int hi) {
  return __builtin_amdgcn_perm(row1, row0, hi ? 0x07060302u : 0x05040100u);
}
__device__ __forceinline__ u64 mix64(u64 e) {
  u64 z = e * 0x9E3779B97F4A7C15ull;
  z = (z ^ (z >> 30)) * 0xBF58476D1CE4E5B9ull;
  z = (z ^ (z >> 27)) * 0x94D049BB133111EBull;
  return z ^ (z >> 31);
}
constexpr int SC1 = 16;

#define UNIT_LOADS(LD, WP, XA, STRIDE, PACE)                                                                \
  const v4u x0 = *reinterpret_cast<const v4u*>((XA) + 16 * ra), x1 = *reinterpret_cast<const v4u*>((XA) + 128 + 16 * ra); \
  const unsigned x2 = *reinterpret_cast<const unsigned*>((XA) + 256 + 4 * ra);                              \
  v4u w[18];                                                                                                \
  {                                                                                                         \
    const unsigned vo8 = (unsigned)(8 * (lane >> 3) * (STRIDE) + 16 * (lane & 7));                          \
    const unsigned vo2 = (unsigned)(2 * (lane >> 3) * (STRIDE) + 16 * (lane & 7));                          \
    _Pragma("unroll") for (int i = 0; i < 18; ++i) {                                                        \
      SB(); if ((PACE) < 32) PWAIT(PACE); SB();                                                             \
      w[i] = i < 16 ? LD((WP) + (size_t)((i >> 3) * 64 + (i & 7)) * (STRIDE) + vo8)                         \
                    : LD((WP) + (size_t)(128 + i - 16) * (STRIDE) + vo2);                                   \
    }                                                                                                       \
    SB();                                                                                                   \
  }
#define UNIT_MFMA()                                                                                         \
  v4f acc[8];                                                                                               \
  _Pragma("unroll") for (int t = 0; t < 8; ++t) {                                                           \
    v4f d = {0.f, 0.f, 0.f, 0.f};                                                                           \
    _Pragma("unroll") for (int sI = 0; sI < 2; ++sI) {                                                      \
      v4u f;                                                                                                \
      _Pragma("unroll") for (int k = 0; k < 4; ++k) f[k] = pair(w[8 * sI + 2 * k + 1][t >> 1], w[8 * sI + 2 * k][t >> 1], t & 1); \
      d = mfma32(sI ? x1 : x0, f, d);                                                                       \
    }                                                                                                       \
    acc[t] = mfma32(v4u{x2, 0u, 0u, 0u}, v4u{pair(w[17][t >> 1], w[16][t >> 1], t & 1), 0u, 0u, 0u}, d);    \
  }

typedef __attribute__((address_space(3))) unsigned lds_u32;
__device__ __forceinline__ void aux_barrier(lds_u32* cnt, unsigned target, int lane) {
  asm volatile("s_waitcnt lgkmcnt(0)" ::: "memory");
  if (lane == 0) __hip_atomic_fetch_add(cnt, 1u, __ATOMIC_RELAXED, __HIP_MEMORY_SCOPE_WORKGROUP);
#pragma unroll 1
  for (unsigned s = 0; s < SPIN_CAP; ++s) {
    const unsigned v = __builtin_amdgcn_readfirstlane(__hip_atomic_load(cnt, __ATOMIC_RELAXED, __HIP_MEMORY_SCOPE_WORKGROUP));
    if (v >= target) break;
    __builtin_amdgcn_s_sleep(2);
  }
  asm volatile("" ::: "memory");
}

__global__ void __launch_bounds__(512, 2)
fused_decode(const char* __restrict__ xg, const char* kc, const char* vc,
             const char* __restrict__ wq, const char* __restrict__ wo, const char* __restrict__ wg,
             const char* __restrict__ wu, const char* __restrict__ wd, const char* __restrict__ gw,
             unsigned* __restrict__ out, char* __restrict__ ws) {
  __shared__ __attribute__((aligned(16))) float lds[L_TOTAL];
  unsigned* ldsu = reinterpret_cast<unsigned*>(lds);
  const int tid = threadIdx.x ^ SWAP, lane = tid & 63;
  const int wave = __builtin_amdgcn_readfirstlane(tid >> 6);
  const int bh = blockIdx.x, b = bh >> 5, h = bh & 31;
  const int r = lane >> 4, c = lane & 15;

  if (wave < 4) {
    const char* kb = kc + (size_t)bh * (SK * DH * 2) + wave * 1024;
    const char* vb = vc + (size_t)bh * (SK * DH * 2) + wave * 1024;
    const unsigned voff = lane * 16;
    v4u a[16 * PARK];
#pragma unroll
    for (int j = 0; j < 16 * PARK; ++j) { if (j >= P1) PWAIT(P1); a[j] = ld_nt(kb + j * 4096 + voff); asm volatile("" ::: "memory"); SB(); }
    wg_barrier();
    const v4u q = *reinterpret_cast<const v4u*>(ldsu + L_QP + 4 * c);
    constexpr float C2 = 0.08838834764831845f * 1.4426950408889634f;
    float* sc = lds + L_SC + wave * 1024 + lane;
    float m = -INFINITY;
#define TR_STEP(HALF, CTRL, BIT)                                                                           \
  _Pragma("unroll") for (int u = 0; u < (HALF); ++u) {                                                      \
    const float lo_ = d[u], hi_ = d[u + (HALF)];                                                            \
    const float t1_ = lo_ + dpp<CTRL>(lo_), t2_ = hi_ + dpp<CTRL>(hi_);                                     \
    d[u] = (c & (BIT)) ? t2_ : t1_;                                                                         \
  }
#define QK_GROUP(GIDX, SLOT0, RELOAD, PFB)                                                                  \
  {                                                                                                         \
    float d[16];                                                                                            \
    _Pragma("unroll") for (int j = 0; j < 16; ++j) {                                                        \
      const v4u t = a[(SLOT0) + j];                                                                         \
      float s = dot2(t[0], q[0], 0.f);                                                                      \
      s = dot2(t[1], q[1], s); s = dot2(t[2], q[2], s); s = dot2(t[3], q[3], s);                            \
      asm volatile("" : "+v"(s)::"memory");                                                                 \
      d[j] = s;                                                                                             \
      SB();                                                                                                 \
      if (RELOAD) { PWAIT(PACE_K); a[(SLOT0) + j] = ld_nt((PFB) + j * 4096 + voff); }                       \
      asm volatile("" ::: "memory");                                                                        \
      SB();                                                                                                 \
    }                                                                                                       \
    TR_STEP(8, XOR8, 8) TR_STEP(4, XOR7, 4) TR_STEP(2, XOR2, 2) TR_STEP(1, XOR1, 1)                         \
    const float s = d[0] * C2;                                                                              \
    m = fmaxf(m, s);                                                                                        \
    sc[(GIDX) * 64] = s;                                                                                    \
  }
    if (PARK > 1) {
      QK_GROUP(0, 0, true, kb + (size_t)PARK * 65536)
#pragma unroll
      for (int gg = 1; gg < PARK; ++gg) QK_GROUP(gg, gg * 16, false, kb)
    }
    for (int it = (PARK > 1 ? PARK : 0); it < 16; ++it) {
      const char* pf = (it + 1 < 16) ? kb + (size_t)(it + 1) * 65536 : vb;
      QK_GROUP(it, 0, true, pf)
      if (it == GATE_IT && tid == 0) __hip_atomic_store((lds_u32*)(ldsu + L_MISC + 1), 1u, __ATOMIC_RELAXED, __HIP_MEMORY_SCOPE_WORKGROUP);
    }
#undef TR_STEP
#undef QK_GROUP
    m = wave_max(m);
    float acc[8] = {0.f, 0.f, 0.f, 0.f, 0.f, 0.f, 0.f, 0.f};
    float l = 0.f;
#define PV_GROUP(GIDX, SLOT0, RELOAD, PFB)                                                                  \
  {                                                                                                         \
    const float p = __builtin_amdgcn_exp2f(sc[(GIDX) * 64] - m);                                            \
    l += p;                                                                                                 \
    float pj;                                                                                               \
    PV_TILE(0, SLOT0, RELOAD, PFB) PV_TILE(1, SLOT0, RELOAD, PFB) PV_TILE(2, SLOT0, RELOAD, PFB)            \
    PV_TILE(3, SLOT0, RELOAD, PFB) PV_TILE(4, SLOT0, RELOAD, PFB) PV_TILE(5, SLOT0, RELOAD, PFB)            \
    PV_TILE(6, SLOT0, RELOAD, PFB) PV_TILE(7, SLOT0, RELOAD, PFB) PV_TILE(8, SLOT0, RELOAD, PFB)            \
    PV_TILE(9, SLOT0, RELOAD, PFB) PV_TILE(10, SLOT0, RELOAD, PFB) PV_TILE(11, SLOT0, RELOAD, PFB)          \
    PV_TILE(12, SLOT0, RELOAD, PFB) PV_TILE(13, SLOT0, RELOAD, PFB) PV_TILE(14, SLOT0, RELOAD, PFB)         \
    PV_TILE(15, SLOT0, RELOAD, PFB)                                                                         \
  }
#define PV_TILE(J, SLOT0, RELOAD, PFB)                                                                      \
  {                                                                                                         \
    const v4u t = a[(SLOT0) + (J)];                                                                         \
    pj = dpp<BCAST + (J)>(p);                                                                               \
    _Pragma("unroll") for (int k = 0; k < 4; ++k) {                                                         \
      acc[2 * k] = fmaf(pj, __builtin_bit_cast(float, t[k] << 16), acc[2 * k]);                             \
      acc[2 * k + 1] = fmaf(pj, __builtin_bit_cast(float, t[k] & 0xffff0000u), acc[2 * k + 1]);             \
    }                                                                                                       \
    asm volatile("" : "+v"(acc[0]), "+v"(acc[1]), "+v"(acc[2]), "+v"(acc[3]), "+v"(acc[4]), "+v"(acc[5]),   \
                 "+v"(acc[6]), "+v"(acc[7])::"memory");                                                     \
    SB();                                                                                                   \
    if (RELOAD) { PWAIT(PACE_V); a[(SLOT0) + (J)] = ld_nt((PFB) + ((SLOT0) + (J)) * 4096 + voff); }         \
    asm volatile("" ::: "memory");                                                                          \
    SB();                                                                                                   \
  }
    for (int it = 0; it < TILES / RING - 1; ++it) {
      const char* pf = vb + (size_t)(it + 1) * (RING * 4096);
#pragma unroll
      for (int gg = 0; gg < RING / 16; ++gg) PV_GROUP(it * (RING / 16) + gg, gg * 16, true, pf)
    }
#pragma unroll
    for (int gg = 0; gg < RING / 16; ++gg) PV_GROUP((TILES / RING - 1) * (RING / 16) + gg, gg * 16, false, vb)
#undef PV_GROUP
#undef PV_TILE
    l = wave_sum(l);
#pragma unroll
    for (int k = 0; k < 8; ++k) acc[k] = xrow_sum(acc[k]);
    float* rec = lds + L_MRG + wave * 132;
    if (r == 0) {
      *reinterpret_cast<v4f*>(rec + 4 + 8 * c) = v4f{acc[0], acc[1], acc[2], acc[3]};
      *reinterpret_cast<v4f*>(rec + 4 + 8 * c + 4) = v4f{acc[4], acc[5], acc[6], acc[7]};
      if (c == 0) { rec[0] = m; rec[1] = l; }
    }
    wg_barrier();
    if (tid < 128) {
      const float* mr = lds + L_MRG;
      const float M = fmaxf(fmaxf(mr[0], mr[132]), fmaxf(mr[264], mr[396]));
      float L = 0.f, o = 0.f;
#pragma unroll
      for (int w = 0; w < 4; ++w) {
        const float e = __builtin_amdgcn_exp2f(mr[w * 132] - M);
        L = fmaf(e, mr[w * 132 + 1], L);
        o = fmaf(e, mr[w * 132 + 4 + tid], o);
      }
      o = o / L;
      const int rg = tid / 44, idx = tid - rg * 44;
      reinterpret_cast<unsigned short*>(ldsu + L_OP)[rg * 48 + idx] = (unsigned short)(cvtpk(o, o) & 0xffffu);
    }
    wg_barrier();
    wg_barrier();
  } else {
    const int aw = wave - 4, ta = tid - 256;
    const int fs = (h & 7) * 4 + (h >> 3);
    const char* xb = xg + b * (DM * 2);
    const __amdgpu_buffer_rsrc_t rs = __builtin_amdgcn_make_buffer_rsrc(ws, 0, WS_TOTAL, 0x00020000);
    const int ra = 2 * r + (c >> 3);
    if (aw == 0) {
      const u64 ep = __hip_atomic_load(reinterpret_cast<u64*>(ws + WS_EPOCH), RLX_AGENT);
      UNIT_LOADS(LDQ, wq + (size_t)(144 * (b >> 1)) * (NH * DH * 2) + h * 256 + (b & 1) * 128, xg + (c & 7) * (DM * 2) + 288 * (b >> 1), NH * DH * 2, 63)
      const unsigned elo = __builtin_amdgcn_readfirstlane((unsigned)ep), ehi = __builtin_amdgcn_readfirstlane((unsigned)(ep >> 32));
      const u64 p1 = (((u64)ehi << 32) | elo) + 1ull;
      const u64 mx = mix64(p1);
      const unsigned thi = (unsigned)(mx >> 32), tlo = (unsigned)p1 << 24;
      SB();
      UNIT_MFMA()
#pragma unroll
      for (int jj = 0; jj < 2; ++jj) {
        float sv[8];
#pragma unroll
        for (int t = 0; t < 8; ++t) {
          auto sw = __builtin_amdgcn_permlane32_swap(__float_as_uint(acc[t][jj]), __float_as_uint(acc[t][2 + jj]), false, false);
          sv[t] = __uint_as_float(sw[0]) + dpp<XOR8>(__uint_as_float(sw[1]));
        }
#pragma unroll
        for (int t = 0; t < 4; ++t) { const float o = dpp<XOR8>(sv[t + 4]); sv[t] = c < 8 ? sv[t] : o; }
        const int cb = 4 * (r & 1) + 2 * (r >> 1) + jj;
#pragma unroll
        for (int u = 0; u < 2; ++u)
          __builtin_amdgcn_raw_buffer_store_b128(
              v4u{(__float_as_uint(sv[2 * u]) >> 8) | tlo, thi, (__float_as_uint(sv[2 * u + 1]) >> 8) | tlo, thi}, rs,
              WS_QIN + (cb * 32 + h) * 4096 + b * 512 + u * 256 + c * 16, 0, SC1);
      }
      if (lane == 0) {
        ldsu[L_MISC + 0] = 0u; ldsu[L_MISC + 1] = 0u;
        ldsu[L_MISC + 2] = elo; ldsu[L_MISC + 3] = ehi;
        ldsu[L_TAG] = ((unsigned)mx & ~0xFFu) | ((unsigned)p1 & 0xFFu);
      }
      if (lane < 4) ldsu[L_OP + 2 * 24 + 20 + lane] = 0u;
      v4u g[4];
      unsigned pend = 0xFu;
      if (QS0) __builtin_amdgcn_s_sleep(QS0);
#pragma unroll 1
      for (unsigned sp = 0; sp < SPIN_CAP; ++sp) {
        asm volatile("" ::: "memory");
#pragma unroll
        for (int p = 0; p < 4; ++p)
          if (pend >> p & 1) g[p] = __builtin_amdgcn_raw_buffer_load_b128(rs, WS_QIN + bh * 4096 + p * 1024 + lane * 16, 0, SC1);
#pragma unroll
        for (int p = 0; p < 4; ++p) {
          const unsigned bad = (((g[p][0] ^ tlo) | (g[p][2] ^ tlo)) >> 24) | (g[p][1] ^ thi) | (g[p][3] ^ thi);
          if (__all(bad == 0)) pend &= ~(1u << p);
        }
        if (pend == 0) break;
        __builtin_amdgcn_s_sleep(QS1);
      }
      float q0 = 0.f, q1 = 0.f;
#pragma unroll
      for (int p = 0; p < 4; ++p) { q0 += __uint_as_float(g[p][0] << 8); q1 += __uint_as_float(g[p][2] << 8); }
      ldsu[L_QP + 32 * (r >> 1) + 4 * (c & 7) + 2 * (c >> 3) + (r & 1)] = cvtpk(q0, q1);
    }
    wg_barrier();
    lds_u32* cnt = (lds_u32*)(ldsu + L_MISC);
    unsigned bar = 0;
    if (bh < 144) {
      const int mat = bh & 1, jc = bh >> 1;
      UNIT_LOADS(LDG, (mat ? wu : wg) + (size_t)(144 * aw) * (DF * 2) + jc * 128, xg + (c & 7) * (DM * 2) + 288 * aw, DF * 2, PAG)
      UNIT_MFMA()
      if ((r >> 1) == (c >> 3)) {
#pragma unroll
        for (int j = 0; j < 4; ++j) {
          float* dst = lds + L_GU + ((aw * 2 + (c >> 3)) * 8 + 4 * (r & 1) + j) * 64 + 8 * (c & 7);
          *reinterpret_cast<v4f*>(dst) = v4f{acc[0][j], acc[1][j], acc[2][j], acc[3][j]};
          *reinterpret_cast<v4f*>(dst + 4) = v4f{acc[4][j], acc[5][j], acc[6][j], acc[7][j]};
        }
      }
      bar += 4;
      aux_barrier(cnt, bar, lane);
      float s0 = 0.f, s1 = 0.f;
#pragma unroll
      for (int p = 0; p < 8; ++p) {
        const v2f v = *reinterpret_cast<const v2f*>(lds + L_GU + p * 512 + 2 * ta);
        s0 += v[0]; s1 += v[1];
      }
      const int pb = ta >> 5;
      __hip_atomic_store(reinterpret_cast<u64*>(ws + WS_GU) + (mat * 8 + pb) * 2304 + jc * 32 + (ta & 31),
                         ((u64)ldsu[L_TAG] << 32) | cvtpk(s0, s1), RLX_AGENT);
    }
    if (aw == 0) {
#pragma unroll 1
      for (unsigned sp = 0; sp < SPIN_CAP; ++sp) {
        if (__builtin_amdgcn_readfirstlane(__hip_atomic_load(cnt + 1, __ATOMIC_RELAXED, __HIP_MEMORY_SCOPE_WORKGROUP))) break;
        __builtin_amdgcn_s_sleep(8);
      }
      const int sl = min(lane, 35);
      const unsigned tg = ldsu[L_TAG];
      const int off = WS_GU + (b * 2304 + fs * 72 + 2 * sl) * 8;
      v4u g, u;
#pragma unroll 1
      for (unsigned sp = 0; sp < SPIN_CAP; ++sp) {
        asm volatile("" ::: "memory");
        g = __builtin_amdgcn_raw_buffer_load_b128(rs, off, 0, SC1);
        u = __builtin_amdgcn_raw_buffer_load_b128(rs, off + 8 * 2304 * 8, 0, SC1);
        if (__all(g[1] == tg && g[3] == tg && u[1] == tg && u[3] == tg)) break;
        __builtin_amdgcn_s_sleep(64);
      }
      float hh[4];
#pragma unroll
      for (int k = 0; k < 4; ++k) {
        const float gs = __uint_as_float((k & 1) ? g[k & 2] & 0xffff0000u : g[k & 2] << 16);
        const float us = __uint_as_float((k & 1) ? u[k & 2] & 0xffff0000u : u[k & 2] << 16);
        const float inner = 0.7978845608028654f * (gs + 0.044715f * gs * gs * gs);
        hh[k] = bf_round(0.5f * gs * (1.f + tanhf(inner))) * us;
      }
      *reinterpret_cast<v2u*>(ldsu + L_H + 2 * sl) = v2u{cvtpk(hh[0], hh[1]), cvtpk(hh[2], hh[3])};
    }
    bar += 4;
    aux_barrier(cnt, bar, lane);
    const int rg = min(ta / 72, 2), cg = ta % 72;
    float accd[8] = {0.f, 0.f, 0.f, 0.f, 0.f, 0.f, 0.f, 0.f};
    {
      const char* wp = wd + (size_t)(fs * 144) * DM * 2;
      const unsigned vo = (unsigned)(48 * rg * DM + 8 * cg) * 2u;
      v4u hv[6];
#pragma unroll
      for (int i = 0; i < 6; ++i) hv[i] = *reinterpret_cast<const v4u*>(ldsu + L_H + 24 * rg + 4 * i);
#pragma unroll
      for (int bt = 0; bt < 2; ++bt) {
        v4u w[24];
#pragma unroll
        for (int i = 0; i < 24; ++i) { AWAIT(i); w[i] = LDD(wp + (size_t)(bt * 24 + i) * (DM * 2) + vo); }
        SB();
#pragma unroll
        for (int i = 0; i < 12; ++i) {
          const int pi = bt * 12 + i;
          gemv2(accd, w[2 * i], w[2 * i + 1], hv[pi >> 2][pi & 3]);
        }
        SB();
      }
    }
    float gate;
    {
      float z = 0.f;
#pragma unroll
      for (int i = 0; i < 5; ++i) {
        const int dw = lane + 64 * i;
        if (dw < 288) z = dot2(reinterpret_cast<const unsigned*>(xb)[dw], reinterpret_cast<const unsigned*>(gw)[dw], z);
      }
      z = wave_sum(z);
      gate = 1.f / (1.f + __expf(-z));
    }
#pragma unroll
    for (int k = 0; k < 8; ++k) accd[k] *= (1.f - gate);
    v4u wv[44];
    {
      const char* wp = wo + (size_t)(h * DH) * DM * 2;
      const unsigned vo = (unsigned)(44 * rg * DM + 8 * cg) * 2u;
      const unsigned vo2 = rg == 2 ? vo - 4u * (DM * 2) : vo;
#pragma unroll
      for (int i = 0; i < 44; ++i) { AWAIT(i); wv[i] = LDO(wp + (size_t)i * (DM * 2) + (i < 40 ? vo : vo2)); }
      SB();
#pragma unroll
      for (int i = 0; i < 22; ++i) {
#pragma unroll
        for (int t = 0; t < 4; ++t) {
          const unsigned w0 = wv[2 * i][t], w1 = wv[2 * i + 1][t];
          wv[2 * i][t] = __builtin_amdgcn_perm(w1, w0, 0x05040100u);
          wv[2 * i + 1][t] = __builtin_amdgcn_perm(w1, w0, 0x07060302u);
        }
      }
      SB();
    }
    wg_barrier();
    wg_barrier();
    {
      v4u ov[6];
#pragma unroll
      for (int i = 0; i < 6; ++i) ov[i] = *reinterpret_cast<const v4u*>(ldsu + L_OP + 24 * rg + 4 * i);
      float acco[8] = {0.f, 0.f, 0.f, 0.f, 0.f, 0.f, 0.f, 0.f};
#pragma unroll
      for (int i = 0; i < 22; ++i) {
        const unsigned op = ov[i >> 2][i & 3];
#pragma unroll
        for (int t = 0; t < 4; ++t) {
          acco[2 * t] = dot2(wv[2 * i][t], op, acco[2 * t]);
          acco[2 * t + 1] = dot2(wv[2 * i + 1][t], op, acco[2 * t + 1]);
        }
      }
      {
        float* dst = lds + L_RED + rg * 576 + 8 * cg;
        *reinterpret_cast<v4f*>(dst) = v4f{fmaf(gate, acco[0], accd[0]), fmaf(gate, acco[1], accd[1]), fmaf(gate, acco[2], accd[2]), fmaf(gate, acco[3], accd[3])};
        *reinterpret_cast<v4f*>(dst + 4) = v4f{fmaf(gate, acco[4], accd[4]), fmaf(gate, acco[5], accd[5]), fmaf(gate, acco[6], accd[6]), fmaf(gate, acco[7], accd[7])};
      }
    }
    wg_barrier();
  }

  const u64 ep1 = (((u64)ldsu[L_MISC + 3] << 32) | ldsu[L_MISC + 2]) + 1ull;
  u64 tag;
  {
    u64 z = ep1 * 0x9E3779B97F4A7C15ull;
    z = (z ^ (z >> 30)) * 0xBF58476D1CE4E5B9ull;
    z = (z ^ (z >> 27)) * 0x94D049BB133111EBull;
    z ^= z >> 31;
    tag = (z & 0xFFFFFFFF00000000ull) | ((ep1 & 0xFFull) << 24);
  }
  u64* inbox = reinterpret_cast<u64*>(ws + WS_INBOX) + (size_t)b * (NH * NH * 18);
  if (tid < 288) {
    const v2f p0 = *reinterpret_cast<const v2f*>(lds + L_RED + 2 * tid);
    const v2f p1 = *reinterpret_cast<const v2f*>(lds + L_RED + 576 + 2 * tid);
    const v2f p2 = *reinterpret_cast<const v2f*>(lds + L_RED + 1152 + 2 * tid);
    const float v0 = p0[0] + p1[0] + p2[0], v1 = p0[1] + p1[1] + p2[1];
    const int hc = tid / 9, j = tid - hc * 9;
    u64* dst = inbox + (size_t)(hc * NH + h) * 18 + 2 * j;
    __hip_atomic_store(dst, tag | (u64)(__float_as_uint(v0) >> 8), RLX_AGENT);
    __hip_atomic_store(dst + 1, tag | (u64)(__float_as_uint(v1) >> 8), RLX_AGENT);
    u64* src = inbox + (size_t)h * (NH * 18) + 2 * tid;
    u64 g0 = 0, g1 = 0;
#pragma unroll 1
    for (unsigned s = 0; s < SPIN_CAP; ++s) {
      g0 = __hip_atomic_load(src, RLX_AGENT);
      g1 = __hip_atomic_load(src + 1, RLX_AGENT);
      if (((g0 ^ tag) >> 24) == 0 && ((g1 ^ tag) >> 24) == 0) break;
      __builtin_amdgcn_s_sleep(1);
    }
    *reinterpret_cast<v2f*>(lds + L_FIN + 2 * tid) =
        v2f{__uint_as_float((unsigned)g0 << 8), __uint_as_float((unsigned)g1 << 8)};
  }
  wg_barrier();
  if (tid < 9) {
    float s0 = 0.f, s1 = 0.f;
#pragma unroll
    for (int p = 0; p < 32; ++p) {
      const v2f v = *reinterpret_cast<const v2f*>(lds + L_FIN + p * 18 + 2 * tid);
      s0 += v[0]; s1 += v[1];
    }
    out[b * (DM / 2) + h * 9 + tid] = cvtpk(s0, s1);
    if (tid == 0 && bh == 0) __hip_atomic_store(reinterpret_cast<u64*>(ws + WS_EPOCH), ep1, RLX_AGENT);
  }
}

extern "C" void kernel_launch(void* const* d_in, const int* in_sizes, int n_in, void* d_out, int out_size, void* d_ws,
                              size_t ws_size, hipStream_t stream) {
  (void)in_sizes; (void)n_in; (void)out_size;
  if (ws_size < (size_t)WS_TOTAL) return;
  fused_decode<<<dim3(NB * NH), dim3(512), 0, stream>>>(
      (const char*)d_in[0], (const char*)d_in[1], (const char*)d_in[2], (const char*)d_in[3], (const char*)d_in[4],
      (const char*)d_in[6], (const char*)d_in[7], (const char*)d_in[8], (const char*)d_in[9], (unsigned*)d_out,
      (char*)d_ws);
}
```

```cpp
#define STAMPS
#define PA 2
#define LDQ ld_nt
#define LDG ld_nt
#define P1 5
#define PK 5
#define CMODE 0
#define DIAG_I 0
#define DIAG_BASE 10000
#define DIAG_SGN 1
#include <hip/hip_runtime.h>
#include <hip/hip_bf16.h>
#include <stdint.h>

typedef unsigned v4u __attribute__((ext_vector_type(4)));
typedef unsigned v2u __attribute__((ext_vector_type(2)));
typedef float v4f __attribute__((ext_vector_type(4)));
typedef float v2f __attribute__((ext_vector_type(2)));
typedef __bf16 bf16x2 __attribute__((ext_vector_type(2)));
typedef unsigned long long u64;

#define RLX_AGENT __ATOMIC_RELAXED, __HIP_MEMORY_SCOPE_AGENT

constexpr int NB = 8, NH = 32, SK = 4096, DH = 128, DM = 576, DF = 4608;
#define RINGD 16
constexpr int RING = RINGD;
constexpr int TILES = 256;
constexpr size_t WS_EPOCH = 0;
constexpr int WS_TS = 4096;
constexpr int WS_INBOX = WS_TS + 32 * 256;
constexpr int WS_GU = WS_INBOX + 8 * 36 * 32 * 128;
constexpr int WS_QIN = WS_GU + 2 * 8 * 2304 * 8;
constexpr int WS_TOTAL = WS_QIN + 256 * 4096;
constexpr unsigned SPIN_CAP = 1u << 18;

constexpr int L_SC = 0;
constexpr int L_QP = L_SC + 4096;
constexpr int L_GU = L_QP + 512;
constexpr int L_H = L_GU + 4096;
constexpr int L_MRG = L_H + 72;
constexpr int L_OP = L_MRG + 4 * 132;
constexpr int L_RED = L_OP + 72;
constexpr int L_MISC = L_RED + 3 * 576;
constexpr int L_TAG = L_MISC + 8;
constexpr int L_TOTAL = L_TAG + 8;

__device__ __forceinline__ float dot2(unsigned a, unsigned b, float acc) {
  return __builtin_amdgcn_fdot2_f32_bf16(__builtin_bit_cast(bf16x2, a), __builtin_bit_cast(bf16x2, b), acc, false);
}
__device__ __forceinline__ unsigned cvtpk(float lo, float hi) {
  unsigned r; asm("v_cvt_pk_bf16_f32 %0, %1, %2" : "=v"(r) : "v"(lo), "v"(hi)); return r;
}
__device__ __forceinline__ float bf_round(float x) { return __builtin_bit_cast(float, cvtpk(x, x) << 16); }
template <int CTRL> __device__ __forceinline__ float dpp(float x) {
  return __builtin_bit_cast(float, __builtin_amdgcn_mov_dpp(__builtin_bit_cast(int, x), CTRL, 0xf, 0xf, true));
}
constexpr int XOR1 = 0xB1, XOR2 = 0x4E, XOR7 = 0x141, XOR8 = 0x128, BCAST = 0x150;
__device__ __forceinline__ float xrow_sum(float x) {
  auto s = __builtin_amdgcn_permlane16_swap(__float_as_uint(x), __float_as_uint(x), false, false);
  x = __uint_as_float(s[0]) + __uint_as_float(s[1]);
  auto t = __builtin_amdgcn_permlane32_swap(__float_as_uint(x), __float_as_uint(x), false, false);
  return __uint_as_float(t[0]) + __uint_as_float(t[1]);
}
__device__ __forceinline__ float xrow_max(float x) {
  auto s = __builtin_amdgcn_permlane16_swap(__float_as_uint(x), __float_as_uint(x), false, false);
  x = fmaxf(__uint_as_float(s[0]), __uint_as_float(s[1]));
  auto t = __builtin_amdgcn_permlane32_swap(__float_as_uint(x), __float_as_uint(x), false, false);
  return fmaxf(__uint_as_float(t[0]), __uint_as_float(t[1]));
}
__device__ __forceinline__ float wave_sum(float x) {
  x += dpp<XOR1>(x); x += dpp<XOR2>(x); x += dpp<XOR7>(x); x += dpp<XOR8>(x);
  return xrow_sum(x);
}
__device__ __forceinline__ float wave_max(float x) {
  x = fmaxf(x, dpp<XOR1>(x)); x = fmaxf(x, dpp<XOR2>(x)); x = fmaxf(x, dpp<XOR7>(x)); x = fmaxf(x, dpp<XOR8>(x));
  return xrow_max(x);
}
__device__ __forceinline__ v4u ld_nt(const char* p) { return __builtin_nontemporal_load(reinterpret_cast<const v4u*>(p)); }
__device__ __forceinline__ v4u ld_w(const char* p) { return *reinterpret_cast<const v4u*>(p); }
__device__ __forceinline__ void wg_barrier() {
  asm volatile("s_waitcnt lgkmcnt(0)" ::: "memory");
  __builtin_amdgcn_s_barrier();
  asm volatile("" ::: "memory");
}
#define SB() __builtin_amdgcn_sched_barrier(0)
#define PV PK
#define PAG PA
#define PARK 2
#define SWAP 256
#define QS0 32
#define QS1 8
#define PHI 7
#define SCH_A 730
#define SCH_K 258
#define SCH_V 235
#define PLO PK
#define SCH_E 50
#define DWAIT(MID, LATE) asm volatile("s_cmp_gt_i32 %0, 0\n\ts_waitcnt vmcnt(%1)\n\ts_cbranch_scc1 1f\n\ts_cmp_gt_i32 %0, %4\n\ts_waitcnt vmcnt(%2)\n\ts_cbranch_scc1 1f\n\ts_waitcnt vmcnt(%3)\n1:" \
    ::"s"(LATE), "n"(PHI - 1), "n"((MID) - 1), "n"(PLO - 1), "n"(-(SCH_E)) : "memory", "scc")
#define NOW() ((unsigned)__builtin_amdgcn_s_memrealtime())
#define RK_D 20
#define RK_T 8
#define RK_I 1
#define RK_W 1
#define RK_O 0
#define RK_B 3
#define RK_J 7
typedef unsigned v16u __attribute__((ext_vector_type(16)));
typedef const __attribute__((address_space(4))) v16u* cv16p;
#define TS0 8
#define TS1 2
#define GATE_IT 5
__device__ __forceinline__ v4u ld_no(const char* p) { return v4u{0x3c003c00u, 0x3c003c00u, 0x3c003c00u, (unsigned)(size_t)p}; }
#define LDD ld_w
#define LDO ld_w
#define STAMP(i) do { SB(); asm volatile("s_memrealtime %0\n\ts_waitcnt lgkmcnt(0)" : "=s"(st[i]) :: "memory"); SB(); } while (0)
constexpr int PACE_K = PK, PACE_V = PV, PACE_A = PA;
#define PWAIT(N) asm volatile("s_waitcnt vmcnt(%0)" ::"n"((N) - 1) : "memory")
#define AWAIT(I) do { SB(); if ((I) >= PACE_A) PWAIT(PACE_A); SB(); } while (0)

__device__ __forceinline__ void gemv2(float (&acc)[8], v4u w0, v4u w1, unsigned xp) {
#pragma unroll
  for (int t = 0; t < 4; ++t) {
    acc[2 * t] = dot2(__builtin_amdgcn_perm(w1[t], w0[t], 0x05040100u), xp, acc[2 * t]);
    acc[2 * t + 1] = dot2(__builtin_amdgcn_perm(w1[t], w0[t], 0x07060302u), xp, acc[2 * t + 1]);
  }
}

typedef __bf16 bf16x8 __attribute__((ext_vector_type(8)));
__device__ __forceinline__ v4f mfma32(v4u a, v4u b, v4f c) {
  return __builtin_amdgcn_mfma_f32_16x16x32_bf16(__builtin_bit_cast(bf16x8, a), __builtin_bit_cast(bf16x8, b), c, 0, 0, 0);
}
__device__ __forceinline__ unsigned pair(unsigned row1, unsigned row0, int hi) {
  return __builtin_amdgcn_perm(row1, row0, hi ? 0x07060302u : 0x05040100u);
}
__device__ __forceinline__ u64 mix64(u64 e) {
  u64 z = e * 0x9E3779B97F4A7C15ull;
  z = (z ^ (z >> 30)) * 0xBF58476D1CE4E5B9ull;
  z = (z ^ (z >> 27)) * 0x94D049BB133111EBull;
  return z ^ (z >> 31);
}
constexpr int SC1 = 16;

#define UNIT_LOADS(LD, WP, XA, STRIDE, PACE)                                                                \
  const v4u x0 = *reinterpret_cast<const v4u*>((XA) + 16 * ra), x1 = *reinterpret_cast<const v4u*>((XA) + 128 + 16 * ra); \
  const unsigned x2 = *reinterpret_cast<const unsigned*>((XA) + 256 + 4 * ra);                              \
  v4u w[18];                                                                                                \
  {                                                                                                         \
    const unsigned vo8 = (unsigned)(8 * (lane >> 3) * (STRIDE) + 16 * (lane & 7));                          \
    const unsigned vo2 = (unsigned)(2 * (lane >> 3) * (STRIDE) + 16 * (lane & 7));                          \
    _Pragma("unroll") for (int i = 0; i < 18; ++i) {                                                        \
      SB(); if ((PACE) < 32) PWAIT(PACE); SB();                                                             \
      w[i] = i < 16 ? LD((WP) + (size_t)((i >> 3) * 64 + (i & 7)) * (STRIDE) + vo8)                         \
                    : LD((WP) + (size_t)(128 + i - 16) * (STRIDE) + vo2);                                   \
    }                                                                                                       \
    SB();                                                                                                   \
  }
#define UNIT_MFMA()                                                                                         \
  v4f acc[8];                                                                                               \
  _Pragma("unroll") for (int t = 0; t < 8; ++t) {                                                           \
    v4f d = {0.f, 0.f, 0.f, 0.f};                                                                           \
    _Pragma("unroll") for (int sI = 0; sI < 2; ++sI) {                                                      \
      v4u f;                                                                                                \
      _Pragma("unroll") for (int k = 0; k < 4; ++k) f[k] = pair(w[8 * sI + 2 * k + 1][t >> 1], w[8 * sI + 2 * k][t >> 1], t & 1); \
      d = mfma32(sI ? x1 : x0, f, d);                                                                       \
    }                                                                                                       \
    acc[t] = mfma32(v4u{x2, 0u, 0u, 0u}, v4u{pair(w[17][t >> 1], w[16][t >> 1], t & 1), 0u, 0u, 0u}, d);    \
  }

typedef __attribute__((address_space(3))) unsigned lds_u32;
__device__ __forceinline__ void aux_barrier(lds_u32* cnt, unsigned target, int lane) {
  asm volatile("s_waitcnt lgkmcnt(0)" ::: "memory");
  if (lane == 0) __hip_atomic_fetch_add(cnt, 1u, __ATOMIC_RELAXED, __HIP_MEMORY_SCOPE_WORKGROUP);
#pragma unroll 1
  for (unsigned s = 0; s < SPIN_CAP; ++s) {
    const unsigned v = __builtin_amdgcn_readfirstlane(__hip_atomic_load(cnt, __ATOMIC_RELAXED, __HIP_MEMORY_SCOPE_WORKGROUP));
    if (v >= target) break;
    __builtin_amdgcn_s_sleep(2);
  }
  asm volatile("" ::: "memory");
}

__global__ void __launch_bounds__(512, 2)
fused_decode(const char* __restrict__ xg, const char* kc, const char* vc,
             const char* __restrict__ wq, const char* __restrict__ wo, const char* __restrict__ wg,
             const char* __restrict__ wu, const char* __restrict__ wd, const char* __restrict__ gw,
             unsigned* __restrict__ out, char* __restrict__ ws) {
  __shared__ __attribute__((aligned(16))) float lds[L_TOTAL];
  unsigned* ldsu = reinterpret_cast<unsigned*>(lds);
  const int tid = threadIdx.x ^ SWAP, lane = tid & 63;
  const int wave = __builtin_amdgcn_readfirstlane(tid >> 6);
  const int bh = blockIdx.x, b = bh >> 5, h = bh & 31;
  const int r = lane >> 4, c = lane & 15;
  u64 st[12] = {0, 0, 0, 0, 0, 0, 0, 0, 0, 0, 0, 0};
  STAMP(0);

  if (wave < 4) {
    const char* kb = kc + (size_t)bh * (SK * DH * 2) + wave * 1024;
    const char* vb = vc + (size_t)bh * (SK * DH * 2) + wave * 1024;
    const unsigned voff = lane * 16;
    unsigned due = NOW() + SCH_A, late = 0, late0 = 0;
#define CK_BEGIN()
#define CK_TILE(J)
    v4u a[16 * PARK];
#pragma unroll
    for (int j = 0; j < 16 * PARK; ++j) { if (j >= P1) PWAIT(P1); a[j] = ld_nt(kb + j * 4096 + voff); asm volatile("" ::: "memory"); SB(); }
    STAMP(11);
    wg_barrier();
    STAMP(1);
    const v4u q = *reinterpret_cast<const v4u*>(ldsu + L_QP + 4 * c);
    constexpr float C2 = 0.08838834764831845f * 1.4426950408889634f;
    float* sc = lds + L_SC + wave * 1024 + lane;
    float m = -INFINITY;
#define TR_STEP(HALF, CTRL, BIT)                                                                           \
  _Pragma("unroll") for (int u = 0; u < (HALF); ++u) {                                                      \
    const float lo_ = d[u], hi_ = d[u + (HALF)];                                                            \
    const float t1_ = lo_ + dpp<CTRL>(lo_), t2_ = hi_ + dpp<CTRL>(hi_);                                     \
    d[u] = (c & (BIT)) ? t2_ : t1_;                                                                         \
  }
#define QK_GROUP(GIDX, SLOT0, RELOAD, PFB, CK)                                                               \
  {                                                                                                         \
    float d[16];                                                                                            \
    _Pragma("unroll") for (int j = 0; j < 16; ++j) {                                                        \
      const v4u t = a[(SLOT0) + j];                                                                         \
      float s = dot2(t[0], q[0], 0.f);                                                                      \
      s = dot2(t[1], q[1], s); s = dot2(t[2], q[2], s); s = dot2(t[3], q[3], s);                            \
      asm volatile("" : "+v"(s)::"memory");                                                                 \
      d[j] = s;                                                                                             \
      SB();                                                                                                 \
      if (RELOAD) { if (CK) { CK_TILE(j) } DWAIT(PACE_K, (CMODE == 2 && j < RK_B) ? late0 : late); a[(SLOT0) + j] = ld_nt((PFB) + j * 4096 + voff); }                 \
      asm volatile("" ::: "memory");                                                                        \
      SB();                                                                                                 \
    }                                                                                                       \
    TR_STEP(8, XOR8, 8) TR_STEP(4, XOR7, 4) TR_STEP(2, XOR2, 2) TR_STEP(1, XOR1, 1)                         \
    const float s = d[0] * C2;                                                                              \
    m = fmaxf(m, s);                                                                                        \
    sc[(GIDX) * 64] = s;                                                                                    \
  }
    if (PARK > 1) {
      QK_GROUP(0, 0, true, kb + (size_t)PARK * 65536, false)
#pragma unroll
      for (int gg = 1; gg < PARK; ++gg) QK_GROUP(gg, gg * 16, false, kb, false)
    }
    for (int it = (PARK > 1 ? PARK : 0); it < 16; ++it) {
      const char* pf = (it + 1 < 16) ? kb + (size_t)(it + 1) * 65536 : vb;
      CK_BEGIN()
      QK_GROUP(it, 0, true, pf, true)
      if (it == GATE_IT && tid == 0) __hip_atomic_store((lds_u32*)(ldsu + L_MISC + 1), 1u, __ATOMIC_RELAXED, __HIP_MEMORY_SCOPE_WORKGROUP);
      if (it == 3) STAMP(2);
    }
#undef TR_STEP
#undef QK_GROUP
    STAMP(3);
    m = wave_max(m);
    float acc[8] = {0.f, 0.f, 0.f, 0.f, 0.f, 0.f, 0.f, 0.f};
    float l = 0.f;
#define PV_GROUP(GIDX, SLOT0, RELOAD, PFB)                                                                  \
  {                                                                                                         \
    const float p = __builtin_amdgcn_exp2f(sc[(GIDX) * 64] - m);                                            \
    l += p;                                                                                                 \
    float pj;                                                                                               \
    PV_TILE(0, SLOT0, RELOAD, PFB) PV_TILE(1, SLOT0, RELOAD, PFB) PV_TILE(2, SLOT0, RELOAD, PFB)            \
    PV_TILE(3, SLOT0, RELOAD, PFB) PV_TILE(4, SLOT0, RELOAD, PFB) PV_TILE(5, SLOT0, RELOAD, PFB)            \
    PV_TILE(6, SLOT0, RELOAD, PFB) PV_TILE(7, SLOT0, RELOAD, PFB) PV_TILE(8, SLOT0, RELOAD, PFB)            \
    PV_TILE(9, SLOT0, RELOAD, PFB) PV_TILE(10, SLOT0, RELOAD, PFB) PV_TILE(11, SLOT0, RELOAD, PFB)          \
    PV_TILE(12, SLOT0, RELOAD, PFB) PV_TILE(13, SLOT0, RELOAD, PFB) PV_TILE(14, SLOT0, RELOAD, PFB)         \
    PV_TILE(15, SLOT0, RELOAD, PFB)                                                                         \
  }
#define PV_TILE(J, SLOT0, RELOAD, PFB)                                                                      \
  {                                                                                                         \
    const v4u t = a[(SLOT0) + (J)];                                                                         \
    pj = dpp<BCAST + (J)>(p);                                                                               \
    _Pragma("unroll") for (int k = 0; k < 4; ++k) {                                                         \
      acc[2 * k] = fmaf(pj, __builtin_bit_cast(float, t[k] << 16), acc[2 * k]);                             \
      acc[2 * k + 1] = fmaf(pj, __builtin_bit_cast(float, t[k] & 0xffff0000u), acc[2 * k + 1]);             \
    }                                                                                                       \
    asm volatile("" : "+v"(acc[0]), "+v"(acc[1]), "+v"(acc[2]), "+v"(acc[3]), "+v"(acc[4]), "+v"(acc[5]),   \
                 "+v"(acc[6]), "+v"(acc[7])::"memory");                                                     \
    SB();                                                                                                   \
    if (RELOAD) { CK_TILE(J) DWAIT(PACE_V, (CMODE == 2 && (J) < RK_B) ? late0 : late); a[(SLOT0) + (J)] = ld_nt((PFB) + ((SLOT0) + (J)) * 4096 + voff); }   \
    asm volatile("" ::: "memory");                                                                          \
    SB();                                                                                                   \
  }
    for (int it = 0; it < TILES / RING - 1; ++it) {
      const char* pf = vb + (size_t)(it + 1) * (RING * 4096);
      CK_BEGIN()
#pragma unroll
      for (int gg = 0; gg < RING / 16; ++gg) PV_GROUP(it * (RING / 16) + gg, gg * 16, true, pf)
      if (it == 0) STAMP(4);
    }
#pragma unroll
    for (int gg = 0; gg < RING / 16; ++gg) PV_GROUP((TILES / RING - 1) * (RING / 16) + gg, gg * 16, false, vb)
#undef PV_GROUP
#undef PV_TILE
    STAMP(5);
    l = wave_sum(l);
#pragma unroll
    for (int k = 0; k < 8; ++k) acc[k] = xrow_sum(acc[k]);
    float* rec = lds + L_MRG + wave * 132;
    if (r == 0) {
      *reinterpret_cast<v4f*>(rec + 4 + 8 * c) = v4f{acc[0], acc[1], acc[2], acc[3]};
      *reinterpret_cast<v4f*>(rec + 4 + 8 * c + 4) = v4f{acc[4], acc[5], acc[6], acc[7]};
      if (c == 0) { rec[0] = m; rec[1] = l; }
    }
    wg_barrier();
    STAMP(6);
    if (tid < 128) {
      const float* mr = lds + L_MRG;
      const float M = fmaxf(fmaxf(mr[0], mr[132]), fmaxf(mr[264], mr[396]));
      float L = 0.f, o = 0.f;
#pragma unroll
      for (int w = 0; w < 4; ++w) {
        const float e = __builtin_amdgcn_exp2f(mr[w * 132] - M);
        L = fmaf(e, mr[w * 132 + 1], L);
        o = fmaf(e, mr[w * 132 + 4 + tid], o);
      }
      o = o / L;
      const int rg = tid / 44, idx = tid - rg * 44;
      reinterpret_cast<unsigned short*>(ldsu + L_OP)[rg * 48 + idx] = (unsigned short)(cvtpk(o, o) & 0xffffu);
    }
    wg_barrier();
    wg_barrier();
    STAMP(7);
  } else {
    const int aw = wave - 4, ta = tid - 256;
    const int fs = (h & 7) * 4 + (h >> 3);
    const char* xb = xg + b * (DM * 2);
    const __amdgpu_buffer_rsrc_t rs = __builtin_amdgcn_make_buffer_rsrc(ws, 0, WS_TOTAL, 0x00020000);
    const int ra = 2 * r + (c >> 3);
    if (aw == 0) {
      const u64 ep = __hip_atomic_load(reinterpret_cast<u64*>(ws + WS_EPOCH), RLX_AGENT);
      UNIT_LOADS(LDQ, wq + (size_t)(144 * (b >> 1)) * (NH * DH * 2) + h * 256 + (b & 1) * 128, xg + (c & 7) * (DM * 2) + 288 * (b >> 1), NH * DH * 2, 63)
      const unsigned elo = __builtin_amdgcn_readfirstlane((unsigned)ep), ehi = __builtin_amdgcn_readfirstlane((unsigned)(ep >> 32));
      const u64 p1 = (((u64)ehi << 32) | elo) + 1ull;
      const u64 mx = mix64(p1);
      const unsigned thi = (unsigned)(mx >> 32), tlo = (unsigned)p1 << 24;
      SB();
      UNIT_MFMA()
#pragma unroll
      for (int jj = 0; jj < 2; ++jj) {
        float sv[8];
#pragma unroll
        for (int t = 0; t < 8; ++t) {
          auto sw = __builtin_amdgcn_permlane32_swap(__float_as_uint(acc[t][jj]), __float_as_uint(acc[t][2 + jj]), false, false);
          sv[t] = __uint_as_float(sw[0]) + dpp<XOR8>(__uint_as_float(sw[1]));
        }
#pragma unroll
        for (int t = 0; t < 4; ++t) { const float o = dpp<XOR8>(sv[t + 4]); sv[t] = c < 8 ? sv[t] : o; }
        const int cb = 4 * (r & 1) + 2 * (r >> 1) + jj;
#pragma unroll
        for (int u = 0; u < 2; ++u)
          __builtin_amdgcn_raw_buffer_store_b128(
              v4u{(__float_as_uint(sv[2 * u]) >> 8) | tlo, thi, (__float_as_uint(sv[2 * u + 1]) >> 8) | tlo, thi}, rs,
              WS_QIN + (cb * 32 + h) * 4096 + b * 512 + u * 256 + c * 16, 0, SC1);
      }
      STAMP(5);
      if (lane == 0) {
        ldsu[L_MISC + 0] = 0u; ldsu[L_MISC + 1] = 0u;
        ldsu[L_MISC + 2] = elo; ldsu[L_MISC + 3] = ehi;
        ldsu[L_TAG] = ((unsigned)mx & ~0xFFu) | ((unsigned)p1 & 0xFFu);
      }
      if (lane < 4) ldsu[L_OP + 2 * 24 + 20 + lane] = 0u;
      v4u g[4];
      unsigned pend = 0xFu;
      if (QS0) __builtin_amdgcn_s_sleep(QS0);
#pragma unroll 1
      for (unsigned sp = 0; sp < SPIN_CAP; ++sp) {
        asm volatile("" ::: "memory");
#pragma unroll
        for (int p = 0; p < 4; ++p)
          if (pend >> p & 1) g[p] = __builtin_amdgcn_raw_buffer_load_b128(rs, WS_QIN + bh * 4096 + p * 1024 + lane * 16, 0, SC1);
#pragma unroll
        for (int p = 0; p < 4; ++p) {
          const unsigned bad = (((g[p][0] ^ tlo) | (g[p][2] ^ tlo)) >> 24) | (g[p][1] ^ thi) | (g[p][3] ^ thi);
          if (__all(bad == 0)) pend &= ~(1u << p);
        }
        if (pend == 0) break;
        __builtin_amdgcn_s_sleep(QS1);
      }
      float q0 = 0.f, q1 = 0.f;
#pragma unroll
      for (int p = 0; p < 4; ++p) { q0 += __uint_as_float(g[p][0] << 8); q1 += __uint_as_float(g[p][2] << 8); }
      ldsu[L_QP + 32 * (r >> 1) + 4 * (c & 7) + 2 * (c >> 3) + (r & 1)] = cvtpk(q0, q1);
      STAMP(10);
    }
    wg_barrier();
    lds_u32* cnt = (lds_u32*)(ldsu + L_MISC);
    STAMP(1);
    unsigned bar = 0;
    if (bh < 144) {
      const int mat = bh & 1, jc = bh >> 1;
      UNIT_LOADS(LDG, (mat ? wu : wg) + (size_t)(144 * aw) * (DF * 2) + jc * 128, xg + (c & 7) * (DM * 2) + 288 * aw, DF * 2, PAG)
      UNIT_MFMA()
      if ((r >> 1) == (c >> 3)) {
#pragma unroll
        for (int j = 0; j < 4; ++j) {
          float* dst = lds + L_GU + ((aw * 2 + (c >> 3)) * 8 + 4 * (r & 1) + j) * 64 + 8 * (c & 7);
          *reinterpret_cast<v4f*>(dst) = v4f{acc[0][j], acc[1][j], acc[2][j], acc[3][j]};
          *reinterpret_cast<v4f*>(dst + 4) = v4f{acc[4][j], acc[5][j], acc[6][j], acc[7][j]};
        }
      }
      bar += 4;
      aux_barrier(cnt, bar, lane);
      float s0 = 0.f, s1 = 0.f;
#pragma unroll
      for (int p = 0; p < 8; ++p) {
        const v2f v = *reinterpret_cast<const v2f*>(lds + L_GU + p * 512 + 2 * ta);
        s0 += v[0]; s1 += v[1];
      }
      const int pb = ta >> 5;
      __hip_atomic_store(reinterpret_cast<u64*>(ws + WS_GU) + (mat * 8 + pb) * 2304 + jc * 32 + (ta & 31),
                         ((u64)ldsu[L_TAG] << 32) | cvtpk(s0, s1), RLX_AGENT);
    }
    if (aw) STAMP(5);
    if (aw == 0) {
#pragma unroll 1
      for (unsigned sp = 0; sp < SPIN_CAP; ++sp) {
        if (__builtin_amdgcn_readfirstlane(__hip_atomic_load(cnt + 1, __ATOMIC_RELAXED, __HIP_MEMORY_SCOPE_WORKGROUP))) break;
        __builtin_amdgcn_s_sleep(8);
      }
      STAMP(11);
      const int sl = min(lane, 35);
      const unsigned tg = ldsu[L_TAG];
      const int off = WS_GU + (b * 2304 + fs * 72 + 2 * sl) * 8;
      v4u g, u;
#pragma unroll 1
      for (unsigned sp = 0; sp < SPIN_CAP / 16; ++sp) {
        asm volatile("" ::: "memory");
        g = __builtin_amdgcn_raw_buffer_load_b128(rs, off, 0, SC1);
        u = __builtin_amdgcn_raw_buffer_load_b128(rs, off + 8 * 2304 * 8, 0, SC1);
        if (__all(g[1] == tg && g[3] == tg && u[1] == tg && u[3] == tg)) break;
        __builtin_amdgcn_s_sleep(64);
      }
      float hh[4];
#pragma unroll
      for (int k = 0; k < 4; ++k) {
        const float gs = __uint_as_float((k & 1) ? g[k & 2] & 0xffff0000u : g[k & 2] << 16);
        const float us = __uint_as_float((k & 1) ? u[k & 2] & 0xffff0000u : u[k & 2] << 16);
        const float inner = 0.7978845608028654f * (gs + 0.044715f * gs * gs * gs);
        hh[k] = bf_round(0.5f * gs * (1.f + tanhf(inner))) * us;
      }
      *reinterpret_cast<v2u*>(ldsu + L_H + 2 * sl) = v2u{cvtpk(hh[0], hh[1]), cvtpk(hh[2], hh[3])};
    }
    bar += 4;
    aux_barrier(cnt, bar, lane);
    STAMP(2);
    const int rg = min(ta / 72, 2), cg = ta % 72;
    float accd[8] = {0.f, 0.f, 0.f, 0.f, 0.f, 0.f, 0.f, 0.f};
    {
      const char* wp = wd + (size_t)(fs * 144) * DM * 2;
      const unsigned vo = (unsigned)(48 * rg * DM + 8 * cg) * 2u;
      v4u hv[6];
#pragma unroll
      for (int i = 0; i < 6; ++i) hv[i] = *reinterpret_cast<const v4u*>(ldsu + L_H + 24 * rg + 4 * i);
#pragma unroll
      for (int bt = 0; bt < 2; ++bt) {
        v4u w[24];
#pragma unroll
        for (int i = 0; i < 24; ++i) { AWAIT(i); w[i] = LDD(wp + (size_t)(bt * 24 + i) * (DM * 2) + vo); }
        SB();
#pragma unroll
        for (int i = 0; i < 12; ++i) {
          const int pi = bt * 12 + i;
          gemv2(accd, w[2 * i], w[2 * i + 1], hv[pi >> 2][pi & 3]);
        }
        SB();
      }
    }
    STAMP(3);
    float gate;
    {
      float z = 0.f;
#pragma unroll
      for (int i = 0; i < 5; ++i) {
        const int dw = lane + 64 * i;
        if (dw < 288) z = dot2(reinterpret_cast<const unsigned*>(xb)[dw], reinterpret_cast<const unsigned*>(gw)[dw], z);
      }
      z = wave_sum(z);
      gate = 1.f / (1.f + __expf(-z));
    }
#pragma unroll
    for (int k = 0; k < 8; ++k) accd[k] *= (1.f - gate);
    v4u wv[44];
    {
      const char* wp = wo + (size_t)(h * DH) * DM * 2;
      const unsigned vo = (unsigned)(44 * rg * DM + 8 * cg) * 2u;
      const unsigned vo2 = rg == 2 ? vo - 4u * (DM * 2) : vo;
#pragma unroll
      for (int i = 0; i < 44; ++i) { AWAIT(i); wv[i] = LDO(wp + (size_t)i * (DM * 2) + (i < 40 ? vo : vo2)); }
      SB();
#pragma unroll
      for (int i = 0; i < 22; ++i) {
#pragma unroll
        for (int t = 0; t < 4; ++t) {
          const unsigned w0 = wv[2 * i][t], w1 = wv[2 * i + 1][t];
          wv[2 * i][t] = __builtin_amdgcn_perm(w1, w0, 0x05040100u);
          wv[2 * i + 1][t] = __builtin_amdgcn_perm(w1, w0, 0x07060302u);
        }
      }
      SB();
    }
    STAMP(4);
    wg_barrier();
    wg_barrier();
    STAMP(6);
    {
      v4u ov[6];
#pragma unroll
      for (int i = 0; i < 6; ++i) ov[i] = *reinterpret_cast<const v4u*>(ldsu + L_OP + 24 * rg + 4 * i);
      float acco[8] = {0.f, 0.f, 0.f, 0.f, 0.f, 0.f, 0.f, 0.f};
#pragma unroll
      for (int i = 0; i < 22; ++i) {
        const unsigned op = ov[i >> 2][i & 3];
#pragma unroll
        for (int t = 0; t < 4; ++t) {
          acco[2 * t] = dot2(wv[2 * i][t], op, acco[2 * t]);
          acco[2 * t + 1] = dot2(wv[2 * i + 1][t], op, acco[2 * t + 1]);
        }
      }
      {
        float* dst = lds + L_RED + rg * 576 + 8 * cg;
        *reinterpret_cast<v4f*>(dst) = v4f{fmaf(gate, acco[0], accd[0]), fmaf(gate, acco[1], accd[1]), fmaf(gate, acco[2], accd[2]), fmaf(gate, acco[3], accd[3])};
        *reinterpret_cast<v4f*>(dst + 4) = v4f{fmaf(gate, acco[4], accd[4]), fmaf(gate, acco[5], accd[5]), fmaf(gate, acco[6], accd[6]), fmaf(gate, acco[7], accd[7])};
      }
    }
    wg_barrier();
    STAMP(7);
  }

  const u64 ep1 = (((u64)ldsu[L_MISC + 3] << 32) | ldsu[L_MISC + 2]) + 1ull;
  u64 tag;
  {
    u64 z = ep1 * 0x9E3779B97F4A7C15ull;
    z = (z ^ (z >> 30)) * 0xBF58476D1CE4E5B9ull;
    z = (z ^ (z >> 27)) * 0x94D049BB133111EBull;
    z ^= z >> 31;
    tag = (z & 0xFFFFFFFF00000000ull) | ((ep1 & 0xFFull) << 24);
  }
  const __amdgpu_buffer_rsrc_t rs = __builtin_amdgcn_make_buffer_rsrc(ws, 0, WS_TOTAL, 0x00020000);
  const unsigned thi = (unsigned)(tag >> 32), tlo = (unsigned)tag;
  if (tid < 288) {
    const v2f p0 = *reinterpret_cast<const v2f*>(lds + L_RED + 2 * tid);
    const v2f p1 = *reinterpret_cast<const v2f*>(lds + L_RED + 576 + 2 * tid);
    const v2f p2 = *reinterpret_cast<const v2f*>(lds + L_RED + 1152 + 2 * tid);
    const float v0 = p0[0] + p1[0] + p2[0], v1 = p0[1] + p1[1] + p2[1];
    __builtin_amdgcn_raw_buffer_store_b128(v4u{(__float_as_uint(v0) >> 8) | tlo, thi, (__float_as_uint(v1) >> 8) | tlo, thi}, rs,
                                           WS_INBOX + ((b * 36 + (tid >> 3)) * 32 + h) * 128 + (tid & 7) * 16, 0, SC1);
  }
  STAMP(8);
  if (wave == 5 || (wave == 6 && h < 4)) {
    const int blk = wave == 5 ? h : 32 + h;
    v4u g[4];
    unsigned pend = 0xFu;
    if (TS0) __builtin_amdgcn_s_sleep(TS0);
#pragma unroll 1
    for (unsigned sp = 0; sp < SPIN_CAP; ++sp) {
      asm volatile("" ::: "memory");
#pragma unroll
      for (int p = 0; p < 4; ++p)
        if (pend >> p & 1) g[p] = __builtin_amdgcn_raw_buffer_load_b128(rs, WS_INBOX + (b * 36 + blk) * 4096 + p * 1024 + lane * 16, 0, SC1);
#pragma unroll
      for (int p = 0; p < 4; ++p) {
        const unsigned bad = (((g[p][0] ^ tlo) | (g[p][2] ^ tlo)) >> 24) | (g[p][1] ^ thi) | (g[p][3] ^ thi);
        if (__all(bad == 0)) pend &= ~(1u << p);
      }
      if (pend == 0) break;
      __builtin_amdgcn_s_sleep(TS1);
    }
    float s0 = 0.f, s1 = 0.f;
#pragma unroll
    for (int p = 0; p < 4; ++p) { s0 += __uint_as_float(g[p][0] << 8); s1 += __uint_as_float(g[p][2] << 8); }
    s0 += dpp<XOR8>(s0); s1 += dpp<XOR8>(s1);
    s0 = xrow_sum(s0); s1 = xrow_sum(s1);
    if (lane < 8) out[b * (DM / 2) + blk * 8 + lane] = cvtpk(s0, s1);
    if (lane == 0 && wave == 5 && bh == 0) __hip_atomic_store(reinterpret_cast<u64*>(ws + WS_EPOCH), ep1, RLX_AGENT);
  }
  STAMP(9);
  if (wave < 4) {
    const unsigned tgt = DIAG_BASE + (DIAG_SGN) * (int)(unsigned)(st[DIAG_I] - st[0]);
#pragma unroll 1
    for (int i = 0; i < (1 << 16); ++i) { if (NOW() - (unsigned)st[0] >= tgt) break; __builtin_amdgcn_s_sleep(2); }
  }
}

extern "C" void kernel_launch(void* const* d_in, const int* in_sizes, int n_in, void* d_out, int out_size, void* d_ws,
                              size_t ws_size, hipStream_t stream) {
  (void)in_sizes; (void)n_in; (void)out_size;
  if (ws_size < (size_t)WS_TOTAL) return;
  fused_decode<<<dim3(NB * NH), dim3(512), 0, stream>>>(
      (const char*)d_in[0], (const char*)d_in[1], (const char*)d_in[2], (const char*)d_in[3], (const char*)d_in[4],
      (const char*)d_in[6], (const char*)d_in[7], (const char*)d_in[8], (const char*)d_in[9], (unsigned*)d_out,
      (char*)d_ws);
}
```

```cpp
#include <hip/hip_runtime.h>
#include <hip/hip_bf16.h>
#include <stdint.h>

typedef unsigned v4u __attribute__((ext_vector_type(4)));
typedef unsigned v2u __attribute__((ext_vector_type(2)));
typedef float v4f __attribute__((ext_vector_type(4)));
typedef float v2f __attribute__((ext_vector_type(2)));
typedef __bf16 bf16x2 __attribute__((ext_vector_type(2)));
typedef unsigned long long u64;

#define RLX_AGENT __ATOMIC_RELAXED, __HIP_MEMORY_SCOPE_AGENT

constexpr int NB = 8, NH = 32, SK = 4096, DH = 128, DM = 576, DF = 4608;
constexpr size_t WS_EPOCH = 0;
constexpr int WS_INBOX = 12288;
constexpr int WS_GU = WS_INBOX + 8 * 36 * 32 * 128;
constexpr int WS_QIN = WS_GU + 2 * 8 * 2304 * 8;
constexpr int WS_TOTAL = WS_QIN + 256 * 4096;
constexpr unsigned SPIN_CAP = 1u << 18;

constexpr int L_SC = 0;
constexpr int L_QP = L_SC + 4096;
constexpr int L_GU = L_QP + 512;
constexpr int L_H = L_GU + 4096;
constexpr int L_MRG = L_H + 72;
constexpr int L_ML = L_MRG + 512;
constexpr int L_RED = L_ML + 8;
constexpr int L_MISC = L_RED + 3 * 576;
constexpr int L_TAG = L_MISC + 8;
constexpr int L_TOTAL = L_TAG + 8;

__device__ __forceinline__ float dot2(unsigned a, unsigned b, float acc) {
  return __builtin_amdgcn_fdot2_f32_bf16(__builtin_bit_cast(bf16x2, a), __builtin_bit_cast(bf16x2, b), acc, false);
}
__device__ __forceinline__ unsigned cvtpk(float lo, float hi) {
  unsigned r; asm("v_cvt_pk_bf16_f32 %0, %1, %2" : "=v"(r) : "v"(lo), "v"(hi)); return r;
}
__device__ __forceinline__ float bf_round(float x) { return __builtin_bit_cast(float, cvtpk(x, x) << 16); }
template <int CTRL> __device__ __forceinline__ float dpp(float x) {
  return __builtin_bit_cast(float, __builtin_amdgcn_mov_dpp(__builtin_bit_cast(int, x), CTRL, 0xf, 0xf, true));
}
constexpr int XOR1 = 0xB1, XOR2 = 0x4E, XOR7 = 0x141, XOR8 = 0x128, BCAST = 0x150;
__device__ __forceinline__ float xrow_sum(float x) {
  auto s = __builtin_amdgcn_permlane16_swap(__float_as_uint(x), __float_as_uint(x), false, false);
  x = __uint_as_float(s[0]) + __uint_as_float(s[1]);
  auto t = __builtin_amdgcn_permlane32_swap(__float_as_uint(x), __float_as_uint(x), false, false);
  return __uint_as_float(t[0]) + __uint_as_float(t[1]);
}
__device__ __forceinline__ float xrow_max(float x) {
  auto s = __builtin_amdgcn_permlane16_swap(__float_as_uint(x), __float_as_uint(x), false, false);
  x = fmaxf(__uint_as_float(s[0]), __uint_as_float(s[1]));
  auto t = __builtin_amdgcn_permlane32_swap(__float_as_uint(x), __float_as_uint(x), false, false);
  return fmaxf(__uint_as_float(t[0]), __uint_as_float(t[1]));
}
__device__ __forceinline__ v2f fold8(const float (&a)[8]) {
  float x[4];
#pragma unroll
  for (int k = 0; k < 4; ++k) {
    auto s = __builtin_amdgcn_permlane32_swap(__float_as_uint(a[k]), __float_as_uint(a[k + 4]), false, false);
    x[k] = __uint_as_float(s[0]) + __uint_as_float(s[1]);
  }
  v2f y;
#pragma unroll
  for (int k = 0; k < 2; ++k) {
    auto s = __builtin_amdgcn_permlane16_swap(__float_as_uint(x[k]), __float_as_uint(x[k + 2]), false, false);
    y[k] = __uint_as_float(s[0]) + __uint_as_float(s[1]);
  }
  return y;
}
__device__ __forceinline__ float wave_sum(float x) {
  x += dpp<XOR1>(x); x += dpp<XOR2>(x); x += dpp<XOR7>(x); x += dpp<XOR8>(x);
  return xrow_sum(x);
}
__device__ __forceinline__ float wave_max(float x) {
  x = fmaxf(x, dpp<XOR1>(x)); x = fmaxf(x, dpp<XOR2>(x)); x = fmaxf(x, dpp<XOR7>(x)); x = fmaxf(x, dpp<XOR8>(x));
  return xrow_max(x);
}
__device__ __forceinline__ v4u ld_nt(const char* p) { return __builtin_nontemporal_load(reinterpret_cast<const v4u*>(p)); }
__device__ __forceinline__ v4u ld_w(const char* p) { return *reinterpret_cast<const v4u*>(p); }
__device__ __forceinline__ void wg_barrier() {
  asm volatile("s_waitcnt lgkmcnt(0)" ::: "memory");
  __builtin_amdgcn_s_barrier();
  asm volatile("" ::: "memory");
}
#define SB() __builtin_amdgcn_sched_barrier(0)
#define PK 5
#define PA 2
#define VEND 2
#define TS0 6
#define QS0 32
#define QS1 8
#define GATE_IT 5
#define TLBP 1
constexpr unsigned OOB = 0x80000000u;
constexpr int PACE_S = PK, PACE_A = PA;
#define PWAIT(N) asm volatile("s_waitcnt vmcnt(%0)" ::"n"((N) - 1) : "memory")
#define DWAIT(OPEN) asm volatile("s_cmp_gt_i32 %0, 0\n\ts_cbranch_scc1 1f\n\ts_waitcnt vmcnt(%1)\n1:" ::"s"(OPEN), "n"(PACE_S - 1) : "memory", "scc")
#define AWAIT(I) do { SB(); if ((I) >= PACE_A) PWAIT(PACE_A); SB(); } while (0)

__device__ __forceinline__ void gemv2(float (&acc)[8], v4u w0, v4u w1, unsigned xp) {
#pragma unroll
  for (int t = 0; t < 4; ++t) {
    acc[2 * t] = dot2(__builtin_amdgcn_perm(w1[t], w0[t], 0x05040100u), xp, acc[2 * t]);
    acc[2 * t + 1] = dot2(__builtin_amdgcn_perm(w1[t], w0[t], 0x07060302u), xp, acc[2 * t + 1]);
  }
}

typedef __bf16 bf16x8 __attribute__((ext_vector_type(8)));
__device__ __forceinline__ v4f mfma32(v4u a, v4u b, v4f c) {
  return __builtin_amdgcn_mfma_f32_16x16x32_bf16(__builtin_bit_cast(bf16x8, a), __builtin_bit_cast(bf16x8, b), c, 0, 0, 0);
}
__device__ __forceinline__ unsigned pair(unsigned row1, unsigned row0, int hi) {
  return __builtin_amdgcn_perm(row1, row0, hi ? 0x07060302u : 0x05040100u);
}
__device__ __forceinline__ u64 mix64(u64 e) {
  u64 z = e * 0x9E3779B97F4A7C15ull;
  z = (z ^ (z >> 30)) * 0xBF58476D1CE4E5B9ull;
  z = (z ^ (z >> 27)) * 0x94D049BB133111EBull;
  return z ^ (z >> 31);
}
constexpr int SC1 = 16;

#define UNIT_LOADS(LD, WP, XA, STRIDE, PACE)                                                                \
  const v4u x0 = *reinterpret_cast<const v4u*>((XA) + 16 * ra), x1 = *reinterpret_cast<const v4u*>((XA) + 128 + 16 * ra); \
  const unsigned x2 = *reinterpret_cast<const unsigned*>((XA) + 256 + 4 * ra);                              \
  v4u w[18];                                                                                                \
  {                                                                                                         \
    const unsigned vo8 = (unsigned)(8 * (lane >> 3) * (STRIDE) + 16 * (lane & 7));                          \
    const unsigned vo2 = (unsigned)(2 * (lane >> 3) * (STRIDE) + 16 * (lane & 7));                          \
    _Pragma("unroll") for (int i = 0; i < 18; ++i) {                                                        \
      SB(); if ((PACE) < 32) PWAIT(PACE); SB();                                                             \
      w[i] = i < 16 ? LD((WP) + (size_t)((i >> 3) * 64 + (i & 7)) * (STRIDE) + vo8)                         \
                    : LD((WP) + (size_t)(128 + i - 16) * (STRIDE) + vo2);                                   \
    }                                                                                                       \
    SB();                                                                                                   \
  }
#define UNIT_MFMA()                                                                                         \
  v4f acc[8];                                                                                               \
  _Pragma("unroll") for (int t = 0; t < 8; ++t) {                                                           \
    v4f d = {0.f, 0.f, 0.f, 0.f};                                                                           \
    _Pragma("unroll") for (int sI = 0; sI < 2; ++sI) {                                                      \
      v4u f;                                                                                                \
      _Pragma("unroll") for (int k = 0; k < 4; ++k) f[k] = pair(w[8 * sI + 2 * k + 1][t >> 1], w[8 * sI + 2 * k][t >> 1], t & 1); \
      d = mfma32(sI ? x1 : x0, f, d);                                                                       \
    }                                                                                                       \
    acc[t] = mfma32(v4u{x2, 0u, 0u, 0u}, v4u{pair(w[17][t >> 1], w[16][t >> 1], t & 1), 0u, 0u, 0u}, d);    \
  }

typedef __attribute__((address_space(3))) unsigned lds_u32;
__device__ __forceinline__ void aux_barrier(lds_u32* cnt, unsigned target, int lane) {
  asm volatile("s_waitcnt lgkmcnt(0)" ::: "memory");
  if (lane == 0) __hip_atomic_fetch_add(cnt, 1u, __ATOMIC_RELAXED, __HIP_MEMORY_SCOPE_WORKGROUP);
#pragma unroll 1
  for (unsigned s = 0; s < SPIN_CAP; ++s) {
    const unsigned v = __builtin_amdgcn_readfirstlane(__hip_atomic_load(cnt, __ATOMIC_RELAXED, __HIP_MEMORY_SCOPE_WORKGROUP));
    if (v >= target) break;
    __builtin_amdgcn_s_sleep(2);
  }
  asm volatile("" ::: "memory");
}

__global__ void __launch_bounds__(512, 2)
fused_decode(const char* __restrict__ xg, const char* kc, const char* vc,
             const char* __restrict__ wq, const char* __restrict__ wo, const char* __restrict__ wg,
             const char* __restrict__ wu, const char* __restrict__ wd, const char* __restrict__ gw,
             unsigned* __restrict__ out, char* __restrict__ ws) {
  __shared__ __attribute__((aligned(16))) float lds[L_TOTAL];
  unsigned* ldsu = reinterpret_cast<unsigned*>(lds);
  const int tid = threadIdx.x ^ 256, lane = tid & 63;
  const int wave = __builtin_amdgcn_readfirstlane(tid >> 6);
  const int bh = blockIdx.x, b = bh >> 5, h = bh & 31;
  const int r = lane >> 4, c = lane & 15;

  if (wave < 4) {
    const char* kb = kc + (size_t)bh * (SK * DH * 2) + wave * 1024;
    const char* vb = vc + (size_t)bh * (SK * DH * 2) + wave * 1024;
    const unsigned voff = lane * 16;
    v4u a[32];
#pragma unroll
    for (int j = 0; j < 32; ++j) { if (j >= PACE_S) PWAIT(PACE_S); a[j] = ld_nt(kb + j * 4096 + voff); asm volatile("" ::: "memory"); SB(); }
    wg_barrier();
    const v4u q = *reinterpret_cast<const v4u*>(ldsu + L_QP + 4 * c);
    constexpr float C2 = 0.08838834764831845f * 1.4426950408889634f;
    float* sc = lds + L_SC + wave * 1024 + lane;
    float m = -INFINITY;
#define TR_STEP(HALF, CTRL, BIT)                                                                           \
  _Pragma("unroll") for (int u = 0; u < (HALF); ++u) {                                                      \
    const float lo_ = d[u], hi_ = d[u + (HALF)];                                                            \
    const float t1_ = lo_ + dpp<CTRL>(lo_), t2_ = hi_ + dpp<CTRL>(hi_);                                     \
    d[u] = (c & (BIT)) ? t2_ : t1_;                                                                         \
  }
#define QK_GROUP(GIDX, SLOT0, RELOAD, PFB)                                                                  \
  {                                                                                                         \
    float d[16];                                                                                            \
    _Pragma("unroll") for (int j = 0; j < 16; ++j) {                                                        \
      const v4u t = a[(SLOT0) + j];                                                                         \
      float s = dot2(t[0], q[0], 0.f);                                                                      \
      s = dot2(t[1], q[1], s); s = dot2(t[2], q[2], s); s = dot2(t[3], q[3], s);                            \
      asm volatile("" : "+v"(s)::"memory");                                                                 \
      d[j] = s;                                                                                             \
      SB();                                                                                                 \
      if (RELOAD) { PWAIT(PACE_S); a[(SLOT0) + j] = ld_nt((PFB) + j * 4096 + voff); }                       \
      asm volatile("" ::: "memory");                                                                        \
      SB();                                                                                                 \
    }                                                                                                       \
    TR_STEP(8, XOR8, 8) TR_STEP(4, XOR7, 4) TR_STEP(2, XOR2, 2) TR_STEP(1, XOR1, 1)                         \
    const float s = d[0] * C2;                                                                              \
    m = fmaxf(m, s);                                                                                        \
    sc[(GIDX) * 64] = s;                                                                                    \
  }
    QK_GROUP(0, 0, true, kb + 2 * 65536)
    QK_GROUP(1, 16, false, kb)
    for (int it = 2; it < 16; ++it) {
      const char* pf = (it + 1 < 16) ? kb + (size_t)(it + 1) * 65536 : vb;
      QK_GROUP(it, 0, true, pf)
      if (it == GATE_IT && tid == 0) __hip_atomic_store((lds_u32*)(ldsu + L_MISC + 1), 1u, __ATOMIC_RELAXED, __HIP_MEMORY_SCOPE_WORKGROUP);
    }
#undef TR_STEP
#undef QK_GROUP
    m = wave_max(m);
    float acc[8] = {0.f, 0.f, 0.f, 0.f, 0.f, 0.f, 0.f, 0.f};
    float pn = __builtin_amdgcn_exp2f(sc[0] - m), l = pn;
#define PV_GROUP(GIDX, SLOT0, RELOAD, PFB)                                                                  \
  {                                                                                                         \
    const float p = pn;                                                                                     \
    if (RELOAD) { pn = __builtin_amdgcn_exp2f(sc[((GIDX) + 1) * 64] - m); l += pn; }                        \
    float pj;                                                                                               \
    PV_TILE(0, SLOT0, RELOAD, PFB) PV_TILE(1, SLOT0, RELOAD, PFB) PV_TILE(2, SLOT0, RELOAD, PFB)            \
    PV_TILE(3, SLOT0, RELOAD, PFB) PV_TILE(4, SLOT0, RELOAD, PFB) PV_TILE(5, SLOT0, RELOAD, PFB)            \
    PV_TILE(6, SLOT0, RELOAD, PFB) PV_TILE(7, SLOT0, RELOAD, PFB) PV_TILE(8, SLOT0, RELOAD, PFB)            \
    PV_TILE(9, SLOT0, RELOAD, PFB) PV_TILE(10, SLOT0, RELOAD, PFB) PV_TILE(11, SLOT0, RELOAD, PFB)          \
    PV_TILE(12, SLOT0, RELOAD, PFB) PV_TILE(13, SLOT0, RELOAD, PFB) PV_TILE(14, SLOT0, RELOAD, PFB)         \
    PV_TILE(15, SLOT0, RELOAD, PFB)                                                                         \
  }
#define PV_TILE(J, SLOT0, RELOAD, PFB)                                                                      \
  {                                                                                                         \
    const v4u t = a[(SLOT0) + (J)];                                                                         \
    pj = dpp<BCAST + (J)>(p);                                                                               \
    _Pragma("unroll") for (int k = 0; k < 4; ++k) {                                                         \
      acc[2 * k] = fmaf(pj, __builtin_bit_cast(float, t[k] << 16), acc[2 * k]);                             \
      acc[2 * k + 1] = fmaf(pj, __builtin_bit_cast(float, t[k] & 0xffff0000u), acc[2 * k + 1]);             \
    }                                                                                                       \
    asm volatile("" : "+v"(acc[0]), "+v"(acc[1]), "+v"(acc[2]), "+v"(acc[3]), "+v"(acc[4]), "+v"(acc[5]),   \
                 "+v"(acc[6]), "+v"(acc[7])::"memory");                                                     \
    SB();                                                                                                   \
    if (RELOAD) { DWAIT(open); a[(SLOT0) + (J)] = ld_nt((PFB) + (J) * 4096 + voff); }                       \
    asm volatile("" ::: "memory");                                                                          \
    SB();                                                                                                   \
  }
    for (int it = 0; it < 15; ++it) {
      const char* pf = vb + (size_t)(it + 1) * 65536;
      const int open = it - (14 - VEND);
      PV_GROUP(it, 0, true, pf)
    }
    l = wave_sum(l);
    if (lane == 0) {
      *reinterpret_cast<v2f*>(lds + L_ML + 2 * wave) = v2f{m, l};
      asm volatile("s_waitcnt lgkmcnt(0)" ::: "memory");
      __hip_atomic_fetch_add((lds_u32*)(ldsu + L_MISC + 6), 1u, __ATOMIC_RELAXED, __HIP_MEMORY_SCOPE_WORKGROUP);
    }
    const unsigned tlo = ldsu[L_MISC + 4], thi = ldsu[L_MISC + 5];
    { const int open = 0; PV_GROUP(15, 0, false, vb) }
#undef PV_GROUP
#undef PV_TILE
    *reinterpret_cast<v2f*>(lds + L_MRG + wave * 128 + 8 * c + 2 * r) = fold8(acc);
    wg_barrier();
    if (wave == 0 || (wave == 1 && h < 4)) {
      const __amdgpu_buffer_rsrc_t rs = __builtin_amdgcn_make_buffer_rsrc(ws, 0, WS_TOTAL, 0x00020000);
      const int blk = 32 * wave + h;
      unsigned off[4], va[4], vb_[4];
#pragma unroll
      for (int p = 0; p < 4; ++p) { off[p] = WS_INBOX + (b * 36 + blk) * 4096 + p * 1024 + lane * 16; va[p] = 0u; vb_[p] = 0u; }
      __builtin_amdgcn_s_sleep(TS0);
#pragma unroll 1
      for (unsigned sp = 0; sp < SPIN_CAP; ++sp) {
        asm volatile("" ::: "memory");
        v4u g[4];
#pragma unroll
        for (int p = 0; p < 4; ++p) g[p] = __builtin_amdgcn_raw_buffer_load_b128(rs, off[p], 0, SC1);
        unsigned all = OOB;
#pragma unroll
        for (int p = 0; p < 4; ++p) {
          const bool ok = ((((g[p][0] ^ tlo) | (g[p][2] ^ tlo)) >> 24) | (g[p][1] ^ thi) | (g[p][3] ^ thi)) == 0u;
          va[p] = ok ? g[p][0] : va[p]; vb_[p] = ok ? g[p][2] : vb_[p]; off[p] = ok ? OOB : off[p];
          all &= off[p];
        }
        if (__all(all != 0u)) break;
      }
      float s0 = 0.f, s1 = 0.f;
#pragma unroll
      for (int p = 0; p < 4; ++p) { s0 += __uint_as_float(va[p] << 8); s1 += __uint_as_float(vb_[p] << 8); }
      s0 += dpp<XOR8>(s0); s1 += dpp<XOR8>(s1);
      s0 = xrow_sum(s0); s1 = xrow_sum(s1);
      if (lane < 8) out[b * (DM / 2) + blk * 8 + lane] = cvtpk(s0, s1);
      if (tid == 0 && bh == 0)
        __hip_atomic_store(reinterpret_cast<u64*>(ws + WS_EPOCH), (((u64)ldsu[L_MISC + 3] << 32) | ldsu[L_MISC + 2]) + 1ull, RLX_AGENT);
    }
  } else {
    const int aw = wave - 4, ta = tid - 256;
    const int fs = (h & 7) * 4 + (h >> 3);
    const char* xb = xg + b * (DM * 2);
    const __amdgpu_buffer_rsrc_t rs = __builtin_amdgcn_make_buffer_rsrc(ws, 0, WS_TOTAL, 0x00020000);
    const int ra = 2 * r + (c >> 3);
    if (aw == 0) {
      const u64 ep = __hip_atomic_load(reinterpret_cast<u64*>(ws + WS_EPOCH), RLX_AGENT);
      UNIT_LOADS(ld_nt, wq + (size_t)(144 * (b >> 1)) * (NH * DH * 2) + h * 256 + (b & 1) * 128, xg + (c & 7) * (DM * 2) + 288 * (b >> 1), NH * DH * 2, 63)
      const unsigned elo = __builtin_amdgcn_readfirstlane((unsigned)ep), ehi = __builtin_amdgcn_readfirstlane((unsigned)(ep >> 32));
      const u64 p1 = (((u64)ehi << 32) | elo) + 1ull;
      const u64 mx = mix64(p1);
      const unsigned thi = (unsigned)(mx >> 32) | 1u, tlo = (unsigned)p1 << 24;
      SB();
      UNIT_MFMA()
#pragma unroll
      for (int jj = 0; jj < 2; ++jj) {
        float sv[8];
#pragma unroll
        for (int t = 0; t < 8; ++t) {
          auto sw = __builtin_amdgcn_permlane32_swap(__float_as_uint(acc[t][jj]), __float_as_uint(acc[t][2 + jj]), false, false);
          sv[t] = __uint_as_float(sw[0]) + dpp<XOR8>(__uint_as_float(sw[1]));
        }
#pragma unroll
        for (int t = 0; t < 4; ++t) { const float o = dpp<XOR8>(sv[t + 4]); sv[t] = c < 8 ? sv[t] : o; }
        const int cb = 4 * (r & 1) + 2 * (r >> 1) + jj;
#pragma unroll
        for (int u = 0; u < 2; ++u)
          __builtin_amdgcn_raw_buffer_store_b128(
              v4u{(__float_as_uint(sv[2 * u]) >> 8) | tlo, thi, (__float_as_uint(sv[2 * u + 1]) >> 8) | tlo, thi}, rs,
              WS_QIN + (cb * 32 + h) * 4096 + b * 512 + u * 256 + c * 16, 0, SC1);
      }
      if (lane == 0) {
        ldsu[L_MISC + 0] = 0u; ldsu[L_MISC + 1] = 0u;
        ldsu[L_MISC + 2] = elo; ldsu[L_MISC + 3] = ehi;
        ldsu[L_MISC + 4] = tlo; ldsu[L_MISC + 5] = thi; ldsu[L_MISC + 6] = 0u;
        ldsu[L_TAG] = ((unsigned)mx & ~0xFFu) | ((unsigned)p1 & 0xFFu);
      }
      v4u g[4];
      unsigned pend = 0xFu;
      __builtin_amdgcn_s_sleep(QS0);
#pragma unroll 1
      for (unsigned sp = 0; sp < SPIN_CAP; ++sp) {
        asm volatile("" ::: "memory");
#pragma unroll
        for (int p = 0; p < 4; ++p)
          if (pend >> p & 1) g[p] = __builtin_amdgcn_raw_buffer_load_b128(rs, WS_QIN + bh * 4096 + p * 1024 + lane * 16, 0, SC1);
#pragma unroll
        for (int p = 0; p < 4; ++p) {
          const unsigned bad = (((g[p][0] ^ tlo) | (g[p][2] ^ tlo)) >> 24) | (g[p][1] ^ thi) | (g[p][3] ^ thi);
          if (__all(bad == 0)) pend &= ~(1u << p);
        }
        if (pend == 0) break;
        __builtin_amdgcn_s_sleep(QS1);
      }
      float q0 = 0.f, q1 = 0.f;
#pragma unroll
      for (int p = 0; p < 4; ++p) { q0 += __uint_as_float(g[p][0] << 8); q1 += __uint_as_float(g[p][2] << 8); }
      ldsu[L_QP + 32 * (r >> 1) + 4 * (c & 7) + 2 * (c >> 3) + (r & 1)] = cvtpk(q0, q1);
    }
    unsigned pft = 0u;
#define TOUCH() { pft = *reinterpret_cast<const unsigned*>(aw == 1 ? wd + (size_t)(fs * 144) * DM * 2 + min(lane * 4096, 144 * DM * 2 - 4) \
                                                                  : wo + (size_t)(h * DH) * DM * 2 + min(lane * 4096, DH * DM * 2 - 4)); }
    if ((TLBP == 1 || (TLBP == 3 && b == 7)) && (aw == 1 || aw == 2)) TOUCH()
    wg_barrier();
    if (TLBP == 2 && b == 7 && (aw == 1 || aw == 2)) TOUCH()
    lds_u32* cnt = (lds_u32*)(ldsu + L_MISC);
    unsigned bar = 0;
    if (bh < 144) {
      const int gu = (bh & 7) * 18 + (bh >> 3), mat = gu >= 72, jc = gu - 72 * mat;
      UNIT_LOADS(ld_nt, (mat ? wu : wg) + (size_t)(144 * aw) * (DF * 2) + jc * 128, xg + (c & 7) * (DM * 2) + 288 * aw, DF * 2, PACE_A)
      UNIT_MFMA()
      if ((r >> 1) == (c >> 3)) {
#pragma unroll
        for (int j = 0; j < 4; ++j) {
          float* dst = lds + L_GU + ((aw * 2 + (c >> 3)) * 8 + 4 * (r & 1) + j) * 64 + 8 * (c & 7);
          *reinterpret_cast<v4f*>(dst) = v4f{acc[0][j], acc[1][j], acc[2][j], acc[3][j]};
          *reinterpret_cast<v4f*>(dst + 4) = v4f{acc[4][j], acc[5][j], acc[6][j], acc[7][j]};
        }
      }
      bar += 4;
      aux_barrier(cnt, bar, lane);
      float s0 = 0.f, s1 = 0.f;
#pragma unroll
      for (int p = 0; p < 8; ++p) {
        const v2f v = *reinterpret_cast<const v2f*>(lds + L_GU + p * 512 + 2 * ta);
        s0 += v[0]; s1 += v[1];
      }
      const int pb = ta >> 5;
      __hip_atomic_store(reinterpret_cast<u64*>(ws + WS_GU) + (mat * 8 + pb) * 2304 + jc * 32 + (ta & 31),
                         ((u64)ldsu[L_TAG] << 32) | cvtpk(s0, s1), RLX_AGENT);
    }
    if (aw == 0) {
#pragma unroll 1
      for (unsigned sp = 0; sp < SPIN_CAP; ++sp) {
        if (__builtin_amdgcn_readfirstlane(__hip_atomic_load(cnt + 1, __ATOMIC_RELAXED, __HIP_MEMORY_SCOPE_WORKGROUP))) break;
        __builtin_amdgcn_s_sleep(8);
      }
      const int sl = min(lane, 35);
      const unsigned tg = ldsu[L_TAG];
      const int off = WS_GU + (b * 2304 + fs * 72 + 2 * sl) * 8;
      v4u g, u;
#pragma unroll 1
      for (unsigned sp = 0; sp < SPIN_CAP / 16; ++sp) {
        asm volatile("" ::: "memory");
        g = __builtin_amdgcn_raw_buffer_load_b128(rs, off, 0, SC1);
        u = __builtin_amdgcn_raw_buffer_load_b128(rs, off + 8 * 2304 * 8, 0, SC1);
        if (__all(g[1] == tg && g[3] == tg && u[1] == tg && u[3] == tg)) break;
        __builtin_amdgcn_s_sleep(64);
      }
      float hh[4];
#pragma unroll
      for (int k = 0; k < 4; ++k) {
        const float gs = __uint_as_float((k & 1) ? g[k & 2] & 0xffff0000u : g[k & 2] << 16);
        const float us = __uint_as_float((k & 1) ? u[k & 2] & 0xffff0000u : u[k & 2] << 16);
        const float inner = 0.7978845608028654f * (gs + 0.044715f * gs * gs * gs);
        hh[k] = bf_round(0.5f * gs * (1.f + tanhf(inner))) * us;
      }
      *reinterpret_cast<v2u*>(ldsu + L_H + 2 * sl) = v2u{cvtpk(hh[0], hh[1]), cvtpk(hh[2], hh[3])};
    }
    bar += 4;
    if (TLBP) asm volatile("" ::"v"(pft));
    aux_barrier(cnt, bar, lane);
    float gate;
    {
      float z = 0.f;
#pragma unroll
      for (int i = 0; i < 5; ++i) {
        const int dw = lane + 64 * i;
        if (dw < 288) z = dot2(reinterpret_cast<const unsigned*>(xb)[dw], reinterpret_cast<const unsigned*>(gw)[dw], z);
      }
      z = wave_sum(z);
      gate = 1.f / (1.f + __expf(-z));
    }
    if (ta < 216) {
      const int rg = ta / 72, cg = ta - rg * 72;
      float accd[8] = {0.f, 0.f, 0.f, 0.f, 0.f, 0.f, 0.f, 0.f};
      const char* wp = wd + (size_t)(fs * 144) * DM * 2;
      const unsigned vo = (unsigned)(48 * rg * DM + 8 * cg) * 2u;
      v4u hv[6];
#pragma unroll
      for (int i = 0; i < 6; ++i) hv[i] = *reinterpret_cast<const v4u*>(ldsu + L_H + 24 * rg + 4 * i);
#pragma unroll
      for (int bt = 0; bt < 2; ++bt) {
        v4u w[24];
#pragma unroll
        for (int i = 0; i < 24; ++i) { AWAIT(i); w[i] = ld_w(wp + (size_t)(bt * 24 + i) * (DM * 2) + vo); }
        SB();
#pragma unroll
        for (int i = 0; i < 12; ++i) {
          const int pi = bt * 12 + i;
          gemv2(accd, w[2 * i], w[2 * i + 1], hv[pi >> 2][pi & 3]);
        }
        SB();
      }
      float* dst = lds + L_RED + rg * 576 + 8 * cg;
      *reinterpret_cast<v4f*>(dst) = v4f{accd[0], accd[1], accd[2], accd[3]};
      *reinterpret_cast<v4f*>(dst + 4) = v4f{accd[4], accd[5], accd[6], accd[7]};
    }
    v4u wv[36];
    {
      const char* wp = wo + (size_t)(h * DH) * DM * 2;
      const unsigned ve = (unsigned)((32 * aw + (lane >> 3)) * (DM * 2) + 1024 + 16 * (lane & 7));
#pragma unroll
      for (int i = 0; i < 4; ++i) { AWAIT(i); wv[32 + i] = ld_w(wp + (size_t)(8 * i) * (DM * 2) + ve); }
      const unsigned vo = (unsigned)(32 * r * (DM * 2) + 256 * aw + 16 * c);
#pragma unroll
      for (int i = 0; i < 32; ++i) { AWAIT(i + 4); wv[i] = ld_w(wp + (size_t)i * (DM * 2) + vo); }
      SB();
#pragma unroll
      for (int i = 0; i < 4; ++i) *reinterpret_cast<v4u*>(ldsu + L_GU + (32 * aw + 8 * i) * 32 + 4 * lane) = wv[32 + i];
      bar += 4;
      aux_barrier(cnt, bar, lane);
#pragma unroll
      for (int i = 0; i < 4; ++i)
        wv[32 + i] = *reinterpret_cast<const v4u*>(ldsu + L_GU + (2 * (lane ^ (4 * (i & 2))) + (i & 1)) * 32 + 8 * aw + 4 * (c >> 3));
#pragma unroll
      for (int i = 0; i < 18; ++i) {
#pragma unroll
        for (int t = 0; t < 4; ++t) {
          const unsigned w0 = wv[2 * i][t], w1 = wv[2 * i + 1][t];
          wv[2 * i][t] = __builtin_amdgcn_perm(w1, w0, 0x05040100u);
          wv[2 * i + 1][t] = __builtin_amdgcn_perm(w1, w0, 0x07060302u);
        }
      }
      SB();
    }
    v2f dm, de;
    {
      const float* p = lds + L_RED + 128 * aw + 8 * c + 2 * r;
      const float* q = lds + L_RED + 512 + 16 * aw + (c & 8) + 2 * r;
      dm = (*reinterpret_cast<const v2f*>(p) + *reinterpret_cast<const v2f*>(p + 576) + *reinterpret_cast<const v2f*>(p + 1152)) * (1.f - gate);
      de = (*reinterpret_cast<const v2f*>(q) + *reinterpret_cast<const v2f*>(q + 576) + *reinterpret_cast<const v2f*>(q + 1152)) * (1.f - gate);
    }
    unsigned tlo = ldsu[L_MISC + 4], thi = ldsu[L_MISC + 5];
    unsigned om = WS_INBOX + ((b * 36 + 8 * aw + (c >> 1)) * 32 + h) * 128 + ((c & 1) * 4 + r) * 16;
    unsigned oe = (lane & 7) ? OOB : WS_INBOX + ((b * 36 + 32 + aw) * 32 + h) * 128 + ((c >> 3) * 4 + r) * 16;
    const float* mrg = lds + L_MRG + 2 * lane;
#define PIN9(I) asm volatile("" : "+v"(wv[I]), "+v"(wv[I + 1]), "+v"(wv[I + 2]), "+v"(wv[I + 3]), "+v"(wv[I + 4]), "+v"(wv[I + 5]), "+v"(wv[I + 6]), "+v"(wv[I + 7]), "+v"(wv[I + 8]));
    PIN9(0) PIN9(9) PIN9(18) PIN9(27)
#undef PIN9
    asm volatile("" : "+v"(tlo), "+v"(thi), "+v"(om), "+v"(oe), "+v"(dm), "+v"(de), "+v"(gate));
#pragma unroll 1
    for (unsigned sp = 0; sp < SPIN_CAP; ++sp) {
      if (__builtin_amdgcn_readfirstlane(__hip_atomic_load(cnt + 6, __ATOMIC_RELAXED, __HIP_MEMORY_SCOPE_WORKGROUP)) >= 4u) break;
      __builtin_amdgcn_s_sleep(4);
    }
    asm volatile("" ::: "memory");
    float f[4];
    {
      const v4f a0 = *reinterpret_cast<const v4f*>(lds + L_ML), a1 = *reinterpret_cast<const v4f*>(lds + L_ML + 4);
      const float M = fmaxf(fmaxf(a0[0], a0[2]), fmaxf(a1[0], a1[2]));
      f[0] = __builtin_amdgcn_exp2f(a0[0] - M); f[1] = __builtin_amdgcn_exp2f(a0[2] - M);
      f[2] = __builtin_amdgcn_exp2f(a1[0] - M); f[3] = __builtin_amdgcn_exp2f(a1[2] - M);
      const float inv = 1.f / (f[0] * a0[1] + f[1] * a0[3] + f[2] * a1[1] + f[3] * a1[3]);
#pragma unroll
      for (int w = 0; w < 4; ++w) f[w] *= inv;
      asm volatile("" : "+v"(f[0]), "+v"(f[1]), "+v"(f[2]), "+v"(f[3]));
    }
    wg_barrier();
    {
      v2f o = {0.f, 0.f};
#pragma unroll
      for (int w = 0; w < 4; ++w) o += *reinterpret_cast<const v2f*>(mrg + w * 128) * f[w];
      const float op = __uint_as_float(cvtpk(o[0], o[1]));
      float am[8] = {0.f, 0.f, 0.f, 0.f, 0.f, 0.f, 0.f, 0.f}, ae[8] = {0.f, 0.f, 0.f, 0.f, 0.f, 0.f, 0.f, 0.f};
#define WO_STEP(A, I, OP)                                                                                   \
  { const unsigned o_ = __float_as_uint(OP);                                                                \
    _Pragma("unroll") for (int t = 0; t < 4; ++t) {                                                         \
      A[2 * t] = dot2(o_, wv[2 * (I)][t], A[2 * t]); A[2 * t + 1] = dot2(o_, wv[2 * (I) + 1][t], A[2 * t + 1]); } }
      WO_STEP(ae, 16, op) WO_STEP(ae, 17, dpp<XOR8>(op))
      v2f e = fold8(ae);
      WO_STEP(am, 0, dpp<BCAST + 0>(op)) WO_STEP(am, 1, dpp<BCAST + 1>(op)) WO_STEP(am, 2, dpp<BCAST + 2>(op)) WO_STEP(am, 3, dpp<BCAST + 3>(op))
      e[0] += dpp<XOR1>(e[0]); e[1] += dpp<XOR1>(e[1]);
      WO_STEP(am, 4, dpp<BCAST + 4>(op)) WO_STEP(am, 5, dpp<BCAST + 5>(op)) WO_STEP(am, 6, dpp<BCAST + 6>(op)) WO_STEP(am, 7, dpp<BCAST + 7>(op))
      e[0] += dpp<XOR2>(e[0]); e[1] += dpp<XOR2>(e[1]);
      WO_STEP(am, 8, dpp<BCAST + 8>(op)) WO_STEP(am, 9, dpp<BCAST + 9>(op)) WO_STEP(am, 10, dpp<BCAST + 10>(op)) WO_STEP(am, 11, dpp<BCAST + 11>(op))
      e[0] += dpp<XOR7>(e[0]); e[1] += dpp<XOR7>(e[1]);
      e = e * gate + de;
      __builtin_amdgcn_raw_buffer_store_b128(v4u{(__float_as_uint(e[0]) >> 8) | tlo, thi, (__float_as_uint(e[1]) >> 8) | tlo, thi}, rs, oe, 0, SC1);
      WO_STEP(am, 12, dpp<BCAST + 12>(op)) WO_STEP(am, 13, dpp<BCAST + 13>(op)) WO_STEP(am, 14, dpp<BCAST + 14>(op)) WO_STEP(am, 15, dpp<BCAST + 15>(op))
#undef WO_STEP
      const v2f v = fold8(am) * gate + dm;
      __builtin_amdgcn_raw_buffer_store_b128(v4u{(__float_as_uint(v[0]) >> 8) | tlo, thi, (__float_as_uint(v[1]) >> 8) | tlo, thi}, rs, om, 0, SC1);
    }
  }
}

extern "C" void kernel_launch(void* const* d_in, const int* in_sizes, int n_in, void* d_out, int out_size, void* d_ws,
                              size_t ws_size, hipStream_t stream) {
  (void)in_sizes; (void)n_in; (void)out_size;
  if (ws_size < (size_t)WS_TOTAL) return;
  fused_decode<<<dim3(NB * NH), dim3(512), 0, stream>>>(
      (const char*)d_in[0], (const char*)d_in[1], (const char*)d_in[2], (const char*)d_in[3], (const char*)d_in[4],
      (const char*)d_in[6], (const char*)d_in[7], (const char*)d_in[8], (const char*)d_in[9], (unsigned*)d_out,
      (char*)d_ws);
}
```

```cpp
#include <hip/hip_runtime.h>
#include <hip/hip_bf16.h>
#include <stdint.h>

typedef unsigned v4u __attribute__((ext_vector_type(4)));
typedef unsigned v2u __attribute__((ext_vector_type(2)));
typedef float v4f __attribute__((ext_vector_type(4)));
typedef float v2f __attribute__((ext_vector_type(2)));
typedef __bf16 bf16x2 __attribute__((ext_vector_type(2)));
typedef unsigned long long u64;

#define RLX_AGENT __ATOMIC_RELAXED, __HIP_MEMORY_SCOPE_AGENT

constexpr int NB = 8, NH = 32, SK = 4096, DH = 128, DM = 576, DF = 4608;
constexpr size_t WS_EPOCH = 0;
constexpr int WS_INBOX = 12288;
constexpr int WS_GU = WS_INBOX + 8 * 36 * 32 * 128;
constexpr int WS_QIN = WS_GU + 2 * 8 * 2304 * 8;
constexpr int WS_TOTAL = WS_QIN + 256 * 4096;
constexpr unsigned SPIN_CAP = 1u << 18;

constexpr int L_SC = 0;
constexpr int L_QP = L_SC + 4096;
constexpr int L_GU = L_QP + 512;
constexpr int L_H = L_GU + 4096;
constexpr int L_MRG = L_H + 72;
constexpr int L_ML = L_MRG + 512;
constexpr int L_RED = L_ML + 8;
constexpr int L_MISC = L_RED + 3 * 576;
constexpr int L_TAG = L_MISC + 8;
constexpr int L_ST = L_TAG + 8;
constexpr int L_TOTAL = L_ST + 4096;

__device__ __forceinline__ float dot2(unsigned a, unsigned b, float acc) {
  return __builtin_amdgcn_fdot2_f32_bf16(__builtin_bit_cast(bf16x2, a), __builtin_bit_cast(bf16x2, b), acc, false);
}
__device__ __forceinline__ unsigned cvtpk(float lo, float hi) {
  unsigned r; asm("v_cvt_pk_bf16_f32 %0, %1, %2" : "=v"(r) : "v"(lo), "v"(hi)); return r;
}
__device__ __forceinline__ float bf_round(float x) { return __builtin_bit_cast(float, cvtpk(x, x) << 16); }
template <int CTRL> __device__ __forceinline__ float dpp(float x) {
  return __builtin_bit_cast(float, __builtin_amdgcn_mov_dpp(__builtin_bit_cast(int, x), CTRL, 0xf, 0xf, true));
}
constexpr int XOR1 = 0xB1, XOR2 = 0x4E, XOR7 = 0x141, XOR8 = 0x128, BCAST = 0x150;
__device__ __forceinline__ float xrow_sum(float x) {
  auto s = __builtin_amdgcn_permlane16_swap(__float_as_uint(x), __float_as_uint(x), false, false);
  x = __uint_as_float(s[0]) + __uint_as_float(s[1]);
  auto t = __builtin_amdgcn_permlane32_swap(__float_as_uint(x), __float_as_uint(x), false, false);
  return __uint_as_float(t[0]) + __uint_as_float(t[1]);
}
__device__ __forceinline__ float xrow_max(float x) {
  auto s = __builtin_amdgcn_permlane16_swap(__float_as_uint(x), __float_as_uint(x), false, false);
  x = fmaxf(__uint_as_float(s[0]), __uint_as_float(s[1]));
  auto t = __builtin_amdgcn_permlane32_swap(__float_as_uint(x), __float_as_uint(x), false, false);
  return fmaxf(__uint_as_float(t[0]), __uint_as_float(t[1]));
}
__device__ __forceinline__ v2f fold8(const float (&a)[8]) {
  float x[4];
#pragma unroll
  for (int k = 0; k < 4; ++k) {
    auto s = __builtin_amdgcn_permlane32_swap(__float_as_uint(a[k]), __float_as_uint(a[k + 4]), false, false);
    x[k] = __uint_as_float(s[0]) + __uint_as_float(s[1]);
  }
  v2f y;
#pragma unroll
  for (int k = 0; k < 2; ++k) {
    auto s = __builtin_amdgcn_permlane16_swap(__float_as_uint(x[k]), __float_as_uint(x[k + 2]), false, false);
    y[k] = __uint_as_float(s[0]) + __uint_as_float(s[1]);
  }
  return y;
}
__device__ __forceinline__ float wave_sum(float x) {
  x += dpp<XOR1>(x); x += dpp<XOR2>(x); x += dpp<XOR7>(x); x += dpp<XOR8>(x);
  return xrow_sum(x);
}
__device__ __forceinline__ float wave_max(float x) {
  x = fmaxf(x, dpp<XOR1>(x)); x = fmaxf(x, dpp<XOR2>(x)); x = fmaxf(x, dpp<XOR7>(x)); x = fmaxf(x, dpp<XOR8>(x));
  return xrow_max(x);
}
__device__ __forceinline__ v4u ld_nt(const char* p) { return __builtin_nontemporal_load(reinterpret_cast<const v4u*>(p)); }
__device__ __forceinline__ v4u ld_w(const char* p) { return *reinterpret_cast<const v4u*>(p); }
__device__ __forceinline__ void wg_barrier() {
  asm volatile("s_waitcnt lgkmcnt(0)" ::: "memory");
  __builtin_amdgcn_s_barrier();
  asm volatile("" ::: "memory");
}
#define SB() __builtin_amdgcn_sched_barrier(0)
#define PK 5
#define PA 2
#define PAG 2
#define GPAT 1
#define QPAT 0
#define QPACE 63
#define LDQ ld_nt
#define LDG ld_nt
#define LD5 ld_nt
#define LDO ld_w
#define LDD ld_w
#define BURST 5
#define ROTD 1
#define ROTO 1
#define VEND 2
#define TS0 6
#define QS0 32
#define QS1 8
constexpr unsigned OOB = 0x80000000u;
constexpr int PACE_S = PK, PACE_A = PA;
#define PWAIT(N) asm volatile("s_waitcnt vmcnt(%0)" ::"n"((N) - 1) : "memory")
#define DWAIT(OPEN) asm volatile("s_cmp_gt_i32 %0, 0\n\ts_cbranch_scc1 1f\n\ts_waitcnt vmcnt(%1)\n1:" ::"s"(OPEN), "n"(PACE_S - 1) : "memory", "scc")
#define AWAIT(I) do { SB(); if ((I) >= PACE_A) PWAIT(PACE_A); SB(); } while (0)

__device__ __forceinline__ void gemv2(float (&acc)[8], v4u w0, v4u w1, unsigned xp) {
#pragma unroll
  for (int t = 0; t < 4; ++t) {
    acc[2 * t] = dot2(__builtin_amdgcn_perm(w1[t], w0[t], 0x05040100u), xp, acc[2 * t]);
    acc[2 * t + 1] = dot2(__builtin_amdgcn_perm(w1[t], w0[t], 0x07060302u), xp, acc[2 * t + 1]);
  }
}

typedef __bf16 bf16x8 __attribute__((ext_vector_type(8)));
__device__ __forceinline__ v4f mfma32(v4u a, v4u b, v4f c) {
  return __builtin_amdgcn_mfma_f32_16x16x32_bf16(__builtin_bit_cast(bf16x8, a), __builtin_bit_cast(bf16x8, b), c, 0, 0, 0);
}
__device__ __forceinline__ unsigned pair(unsigned row1, unsigned row0, int hi) {
  return __builtin_amdgcn_perm(row1, row0, hi ? 0x07060302u : 0x05040100u);
}
__device__ __forceinline__ u64 mix64(u64 e) {
  u64 z = e * 0x9E3779B97F4A7C15ull;
  z = (z ^ (z >> 30)) * 0xBF58476D1CE4E5B9ull;
  z = (z ^ (z >> 27)) * 0x94D049BB133111EBull;
  return z ^ (z >> 31);
}
constexpr int SC1 = 16;

#define UNIT_LOADS(LD, WP, XA, STRIDE, PACE, PAT, SLOW)                                                     \
  v4u x0, x1;                                                                                               \
  unsigned x2;                                                                                              \
  if ((PAT) == 2) {                                                                                         \
    _Pragma("unroll") for (int j = 0; j < 4; ++j) {                                                         \
      x0[j] = *reinterpret_cast<const unsigned*>((XA) + 4 * ra + 32 * j);                                   \
      x1[j] = *reinterpret_cast<const unsigned*>((XA) + 128 + 4 * ra + 32 * j);                             \
    }                                                                                                       \
    x2 = *reinterpret_cast<const unsigned*>((XA) + 256 + 4 * ra);                                           \
  } else if ((PAT) == 3) {                                                                                  \
    const unsigned short* xs_ = reinterpret_cast<const unsigned short*>((XA) + 2 * ra);                     \
    _Pragma("unroll") for (int j = 0; j < 4; ++j) {                                                         \
      x0[j] = xs_[16 * j] | (unsigned)xs_[16 * j + 8] << 16;                                                \
      x1[j] = xs_[64 + 16 * j] | (unsigned)xs_[64 + 16 * j + 8] << 16;                                      \
    }                                                                                                       \
    x2 = xs_[128] | (unsigned)xs_[136] << 16;                                                               \
  } else {                                                                                                  \
    x0 = *reinterpret_cast<const v4u*>((XA) + 16 * ra); x1 = *reinterpret_cast<const v4u*>((XA) + 128 + 16 * ra); \
    x2 = *reinterpret_cast<const unsigned*>((XA) + 256 + 4 * ra);                                           \
  }                                                                                                         \
  v4u w[18];                                                                                                \
  {                                                                                                         \
    const unsigned vo8 = (unsigned)(8 * (lane >> 3) * (STRIDE) + 16 * (lane & 7));                          \
    const unsigned vo2 = (unsigned)(2 * (lane >> 3) * (STRIDE) + 16 * (lane & 7));                          \
    const unsigned vo1 = (unsigned)((lane >> 3) * (STRIDE) + 16 * (lane & 7));                              \
    _Pragma("unroll") for (int i = 0; i < 18; ++i) {                                                        \
      SB(); if ((PACE) < 32) PWAIT(PACE);                                                                   \
      if ((PAT) == 1) asm volatile("s_cmp_lg_u32 %0, 3\n\ts_cbranch_scc1 1f\n\ts_waitcnt vmcnt(0)\n1:" ::"s"(SLOW) : "memory", "scc"); \
      SB();                                                                                                 \
      w[i] = LD((WP) + (size_t)((PAT) == 2 ? 16 * (i >> 1) + (i & 1) : (PAT) == 3 ? 8 * i : i < 16 ? (i >> 3) * 64 + (i & 7) : 112 + i) * (STRIDE) + \
                ((PAT) == 3 ? vo1 : (PAT) == 2 || i >= 16 ? vo2 : vo8));                                    \
    }                                                                                                       \
    SB();                                                                                                   \
  }
#define UNIT_MFMA()                                                                                         \
  v4f acc[8];                                                                                               \
  _Pragma("unroll") for (int t = 0; t < 8; ++t) {                                                           \
    v4f d = {0.f, 0.f, 0.f, 0.f};                                                                           \
    _Pragma("unroll") for (int sI = 0; sI < 2; ++sI) {                                                      \
      v4u f;                                                                                                \
      _Pragma("unroll") for (int k = 0; k < 4; ++k) f[k] = pair(w[8 * sI + 2 * k + 1][t >> 1], w[8 * sI + 2 * k][t >> 1], t & 1); \
      d = mfma32(sI ? x1 : x0, f, d);                                                                       \
    }                                                                                                       \
    acc[t] = mfma32(v4u{x2, 0u, 0u, 0u}, v4u{pair(w[17][t >> 1], w[16][t >> 1], t & 1), 0u, 0u, 0u}, d);    \
  }

typedef __attribute__((address_space(3))) unsigned lds_u32;
__device__ __forceinline__ void aux_barrier(lds_u32* cnt, unsigned target, int lane) {
  asm volatile("s_waitcnt lgkmcnt(0)" ::: "memory");
  if (lane == 0) __hip_atomic_fetch_add(cnt, 1u, __ATOMIC_RELAXED, __HIP_MEMORY_SCOPE_WORKGROUP);
#pragma unroll 1
  for (unsigned s = 0; s < SPIN_CAP; ++s) {
    const unsigned v = __builtin_amdgcn_readfirstlane(__hip_atomic_load(cnt, __ATOMIC_RELAXED, __HIP_MEMORY_SCOPE_WORKGROUP));
    if (v >= target) break;
    __builtin_amdgcn_s_sleep(2);
  }
  asm volatile("" ::: "memory");
}

__global__ void __launch_bounds__(512, 2)
fused_decode(const char* __restrict__ xg, const char* kc, const char* vc,
             const char* __restrict__ wq, const char* __restrict__ wo, const char* __restrict__ wg,
             const char* __restrict__ wu, const char* __restrict__ wd, const char* __restrict__ gw,
             unsigned* __restrict__ out, char* __restrict__ ws) {
  __shared__ __attribute__((aligned(16))) float lds[L_TOTAL];
  unsigned* ldsu = reinterpret_cast<unsigned*>(lds);
  const int tid = threadIdx.x ^ 256, lane = tid & 63;
  const int wave = __builtin_amdgcn_readfirstlane(tid >> 6);
  const int bh = blockIdx.x, b = bh >> 5, h = bh & 31;
  const int r = lane >> 4, c = lane & 15;

  if (wave < 4) {
    const char* kb = kc + (size_t)bh * (SK * DH * 2) + wave * 1024;
    const char* vb = vc + (size_t)bh * (SK * DH * 2) + wave * 1024;
    const unsigned voff = lane * 16;
    v4u a[32];
#pragma unroll
    for (int j = 0; j < BURST; ++j) { a[j] = LD5(kb + j * 4096 + voff); asm volatile("" ::: "memory"); SB(); }
#pragma unroll
    for (int j = BURST; j < 32; ++j) { PWAIT(PACE_S); a[j] = ld_nt(kb + j * 4096 + voff); asm volatile("" ::: "memory"); SB(); }
    wg_barrier();
    const v4u q = *reinterpret_cast<const v4u*>(ldsu + L_QP + 4 * c);
    constexpr float C2 = 0.08838834764831845f * 1.4426950408889634f;
    float* sc = lds + L_SC + wave * 1024 + lane;
    float m = -INFINITY;
#define TR_STEP(HALF, CTRL, BIT)                                                                           \
  _Pragma("unroll") for (int u = 0; u < (HALF); ++u) {                                                      \
    const float lo_ = d[u], hi_ = d[u + (HALF)];                                                            \
    const float t1_ = lo_ + dpp<CTRL>(lo_), t2_ = hi_ + dpp<CTRL>(hi_);                                     \
    d[u] = (c & (BIT)) ? t2_ : t1_;                                                                         \
  }
#define QK_GROUP(GIDX, SLOT0, RELOAD, PFB)                                                                  \
  {                                                                                                         \
    float d[16];                                                                                            \
    _Pragma("unroll") for (int j = 0; j < 16; ++j) {                                                        \
      const v4u t = a[(SLOT0) + j];                                                                         \
      float s = dot2(t[0], q[0], 0.f);                                                                      \
      s = dot2(t[1], q[1], s); s = dot2(t[2], q[2], s); s = dot2(t[3], q[3], s);                            \
      asm volatile("" : "+v"(s)::"memory");                                                                 \
      d[j] = s;                                                                                             \
      SB();                                                                                                 \
      if (RELOAD) { PWAIT(PACE_S); a[(SLOT0) + j] = ld_nt((PFB) + j * 4096 + voff); }                       \
      asm volatile("" ::: "memory");                                                                        \
      SB();                                                                                                 \
    }                                                                                                       \
    TR_STEP(8, XOR8, 8) TR_STEP(4, XOR7, 4) TR_STEP(2, XOR2, 2) TR_STEP(1, XOR1, 1)                         \
    const float s = d[0] * C2;                                                                              \
    m = fmaxf(m, s);                                                                                        \
    sc[(GIDX) * 64] = s;                                                                                    \
  }
    QK_GROUP(0, 0, true, kb + 2 * 65536)
    QK_GROUP(1, 16, false, kb)
    for (int it = 2; it < 16; ++it) {
      const char* pf = (it + 1 < 16) ? kb + (size_t)(it + 1) * 65536 : vb;
      QK_GROUP(it, 0, true, pf)
    }
#undef TR_STEP
#undef QK_GROUP
    m = wave_max(m);
    float acc[8] = {0.f, 0.f, 0.f, 0.f, 0.f, 0.f, 0.f, 0.f};
    float pn = __builtin_amdgcn_exp2f(sc[0] - m), l = pn;
#define PV_GROUP(GIDX, SLOT0, RELOAD, PFB)                                                                  \
  {                                                                                                         \
    const float p = pn;                                                                                     \
    if (RELOAD) { pn = __builtin_amdgcn_exp2f(sc[((GIDX) + 1) * 64] - m); l += pn; }                        \
    float pj;                                                                                               \
    PV_TILE(0, SLOT0, RELOAD, PFB) PV_TILE(1, SLOT0, RELOAD, PFB) PV_TILE(2, SLOT0, RELOAD, PFB)            \
    PV_TILE(3, SLOT0, RELOAD, PFB) PV_TILE(4, SLOT0, RELOAD, PFB) PV_TILE(5, SLOT0, RELOAD, PFB)            \
    PV_TILE(6, SLOT0, RELOAD, PFB) PV_TILE(7, SLOT0, RELOAD, PFB) PV_TILE(8, SLOT0, RELOAD, PFB)            \
    PV_TILE(9, SLOT0, RELOAD, PFB) PV_TILE(10, SLOT0, RELOAD, PFB) PV_TILE(11, SLOT0, RELOAD, PFB)          \
    PV_TILE(12, SLOT0, RELOAD, PFB) PV_TILE(13, SLOT0, RELOAD, PFB) PV_TILE(14, SLOT0, RELOAD, PFB)         \
    PV_TILE(15, SLOT0, RELOAD, PFB)                                                                         \
  }
#define PV_TILE(J, SLOT0, RELOAD, PFB)                                                                      \
  {                                                                                                         \
    const v4u t = a[(SLOT0) + (J)];                                                                         \
    pj = dpp<BCAST + (J)>(p);                                                                               \
    _Pragma("unroll") for (int k = 0; k < 4; ++k) {                                                         \
      acc[2 * k] = fmaf(pj, __builtin_bit_cast(float, t[k] << 16), acc[2 * k]);                             \
      acc[2 * k + 1] = fmaf(pj, __builtin_bit_cast(float, t[k] & 0xffff0000u), acc[2 * k + 1]);             \
    }                                                                                                       \
    asm volatile("" : "+v"(acc[0]), "+v"(acc[1]), "+v"(acc[2]), "+v"(acc[3]), "+v"(acc[4]), "+v"(acc[5]),   \
                 "+v"(acc[6]), "+v"(acc[7])::"memory");                                                     \
    SB();                                                                                                   \
    if (RELOAD) { DWAIT(open); a[(SLOT0) + (J)] = ld_nt((PFB) + (J) * 4096 + voff); }                       \
    asm volatile("" ::: "memory");                                                                          \
    SB();                                                                                                   \
  }
    for (int it = 0; it < 15; ++it) {
      const char* pf = vb + (size_t)(it + 1) * 65536;
      const int open = it - (14 - VEND);
      PV_GROUP(it, 0, true, pf)
    }
    l = wave_sum(l);
    if (lane == 0) {
      *reinterpret_cast<v2f*>(lds + L_ML + 2 * wave) = v2f{m, l};
      asm volatile("s_waitcnt lgkmcnt(0)" ::: "memory");
      __hip_atomic_fetch_add((lds_u32*)(ldsu + L_MISC + 6), 1u, __ATOMIC_RELAXED, __HIP_MEMORY_SCOPE_WORKGROUP);
    }
    const unsigned tlo = ldsu[L_MISC + 4], thi = ldsu[L_MISC + 5];
    { const int open = 0; PV_GROUP(15, 0, false, vb) }
#undef PV_GROUP
#undef PV_TILE
    *reinterpret_cast<v2f*>(lds + L_MRG + wave * 128 + 8 * c + 2 * r) = fold8(acc);
    wg_barrier();
    if (wave == 0 || (wave == 1 && h < 4)) {
      const __amdgpu_buffer_rsrc_t rs = __builtin_amdgcn_make_buffer_rsrc(ws, 0, WS_TOTAL, 0x00020000);
      const int blk = 32 * wave + h;
      unsigned off[4], va[4], vb_[4];
#pragma unroll
      for (int p = 0; p < 4; ++p) { off[p] = WS_INBOX + (b * 36 + blk) * 4096 + p * 1024 + lane * 16; va[p] = 0u; vb_[p] = 0u; }
      __builtin_amdgcn_s_sleep(TS0);
#pragma unroll 1
      for (unsigned sp = 0; sp < SPIN_CAP; ++sp) {
        asm volatile("" ::: "memory");
        v4u g[4];
#pragma unroll
        for (int p = 0; p < 4; ++p) g[p] = __builtin_amdgcn_raw_buffer_load_b128(rs, off[p], 0, SC1);
        unsigned all = OOB;
#pragma unroll
        for (int p = 0; p < 4; ++p) {
          const bool ok = ((((g[p][0] ^ tlo) | (g[p][2] ^ tlo)) >> 24) | (g[p][1] ^ thi) | (g[p][3] ^ thi)) == 0u;
          va[p] = ok ? g[p][0] : va[p]; vb_[p] = ok ? g[p][2] : vb_[p]; off[p] = ok ? OOB : off[p];
          all &= off[p];
        }
        if (__all(all != 0u)) break;
      }
      float s0 = 0.f, s1 = 0.f;
#pragma unroll
      for (int p = 0; p < 4; ++p) { s0 += __uint_as_float(va[p] << 8); s1 += __uint_as_float(vb_[p] << 8); }
      s0 += dpp<XOR8>(s0); s1 += dpp<XOR8>(s1);
      s0 = xrow_sum(s0); s1 = xrow_sum(s1);
      if (lane < 8) out[b * (DM / 2) + blk * 8 + lane] = cvtpk(s0, s1);
      if (tid == 0 && bh == 0)
        __hip_atomic_store(reinterpret_cast<u64*>(ws + WS_EPOCH), (((u64)ldsu[L_MISC + 3] << 32) | ldsu[L_MISC + 2]) + 1ull, RLX_AGENT);
    }
  } else {
    const int aw = wave - 4, ta = tid - 256;
    const int fs = (h & 7) * 4 + (h >> 3);
    const char* xb = xg + b * (DM * 2);
    const __amdgpu_buffer_rsrc_t rs = __builtin_amdgcn_make_buffer_rsrc(ws, 0, WS_TOTAL, 0x00020000);
    const int ra = 2 * r + (c >> 3);
    if (aw == 0) {
      const u64 ep = __hip_atomic_load(reinterpret_cast<u64*>(ws + WS_EPOCH), RLX_AGENT);
      UNIT_LOADS(LDQ, wq + (size_t)(144 * (b >> 1)) * (NH * DH * 2) + h * 256 + (b & 1) * 128, xg + (c & 7) * (DM * 2) + 288 * (b >> 1), NH * DH * 2, QPACE, QPAT, 0)
      const unsigned elo = __builtin_amdgcn_readfirstlane((unsigned)ep), ehi = __builtin_amdgcn_readfirstlane((unsigned)(ep >> 32));
      const u64 p1 = (((u64)ehi << 32) | elo) + 1ull;
      const u64 mx = mix64(p1);
      const unsigned thi = (unsigned)(mx >> 32) | 1u, tlo = (unsigned)p1 << 24;
      SB();
      UNIT_MFMA()
#pragma unroll
      for (int jj = 0; jj < 2; ++jj) {
        float sv[8];
#pragma unroll
        for (int t = 0; t < 8; ++t) {
          auto sw = __builtin_amdgcn_permlane32_swap(__float_as_uint(acc[t][jj]), __float_as_uint(acc[t][2 + jj]), false, false);
          sv[t] = __uint_as_float(sw[0]) + dpp<XOR8>(__uint_as_float(sw[1]));
        }
#pragma unroll
        for (int t = 0; t < 4; ++t) { const float o = dpp<XOR8>(sv[t + 4]); sv[t] = c < 8 ? sv[t] : o; }
        const int cb = 4 * (r & 1) + 2 * (r >> 1) + jj;
#pragma unroll
        for (int u = 0; u < 2; ++u)
          __builtin_amdgcn_raw_buffer_store_b128(
              v4u{(__float_as_uint(sv[2 * u]) >> 8) | tlo, thi, (__float_as_uint(sv[2 * u + 1]) >> 8) | tlo, thi}, rs,
              WS_QIN + (cb * 32 + h) * 4096 + b * 512 + u * 256 + c * 16, 0, SC1);
      }
      if (lane == 0) {
        ldsu[L_MISC + 0] = 0u;
        ldsu[L_MISC + 2] = elo; ldsu[L_MISC + 3] = ehi;
        ldsu[L_MISC + 4] = tlo; ldsu[L_MISC + 5] = thi; ldsu[L_MISC + 6] = 0u;
        ldsu[L_TAG] = ((unsigned)mx & ~0xFFu) | ((unsigned)p1 & 0xFFu);
      }
      v4u g[4];
      unsigned pend = 0xFu;
      __builtin_amdgcn_s_sleep(QS0);
#pragma unroll 1
      for (unsigned sp = 0; sp < SPIN_CAP; ++sp) {
        asm volatile("" ::: "memory");
#pragma unroll
        for (int p = 0; p < 4; ++p)
          if (pend >> p & 1) g[p] = __builtin_amdgcn_raw_buffer_load_b128(rs, WS_QIN + bh * 4096 + p * 1024 + lane * 16, 0, SC1);
#pragma unroll
        for (int p = 0; p < 4; ++p) {
          const unsigned bad = (((g[p][0] ^ tlo) | (g[p][2] ^ tlo)) >> 24) | (g[p][1] ^ thi) | (g[p][3] ^ thi);
          if (__all(bad == 0)) pend &= ~(1u << p);
        }
        if (pend == 0) break;
        __builtin_amdgcn_s_sleep(QS1);
      }
      float q0 = 0.f, q1 = 0.f;
#pragma unroll
      for (int p = 0; p < 4; ++p) { q0 += __uint_as_float(g[p][0] << 8); q1 += __uint_as_float(g[p][2] << 8); }
      ldsu[L_QP + 32 * (r >> 1) + 4 * (c & 7) + 2 * (c >> 3) + (r & 1)] = cvtpk(q0, q1);
    }
    wg_barrier();
    lds_u32* cnt = (lds_u32*)(ldsu + L_MISC);
    unsigned bar = 0;
    if (bh < 144) {
      const int gu = (bh & 7) * 18 + (bh >> 3), mat = gu >= 72, jc = gu - 72 * mat;
      UNIT_LOADS(LDG, (mat ? wu : wg) + (size_t)(144 * aw) * (DF * 2) + jc * 128, xg + (c & 7) * (DM * 2) + 288 * aw, DF * 2, PAG, GPAT, (jc & 7))
      UNIT_MFMA()
      if ((r >> 1) == (c >> 3)) {
#pragma unroll
        for (int j = 0; j < 4; ++j) {
          float* dst = lds + L_GU + ((aw * 2 + (c >> 3)) * 8 + 4 * (r & 1) + j) * 64 + 8 * (c & 7);
          *reinterpret_cast<v4f*>(dst) = v4f{acc[0][j], acc[1][j], acc[2][j], acc[3][j]};
          *reinterpret_cast<v4f*>(dst + 4) = v4f{acc[4][j], acc[5][j], acc[6][j], acc[7][j]};
        }
      }
      bar += 4;
      aux_barrier(cnt, bar, lane);
      float s0 = 0.f, s1 = 0.f;
#pragma unroll
      for (int p = 0; p < 8; ++p) {
        const v2f v = *reinterpret_cast<const v2f*>(lds + L_GU + p * 512 + 2 * ta);
        s0 += v[0]; s1 += v[1];
      }
      const int pb = ta >> 5;
      __hip_atomic_store(reinterpret_cast<u64*>(ws + WS_GU) + (mat * 8 + pb) * 2304 + jc * 32 + (ta & 31),
                         ((u64)ldsu[L_TAG] << 32) | cvtpk(s0, s1), RLX_AGENT);
    }
    const int rk = (7 - b) * (h < 16 ? 4 : 3);
    const int ro = ROTO * (rk * 4 / 3);
    const int rl = 16 * r + ((c + ro) & 15);
    v4u wv[36];
    {
      const char* wp = wo + (size_t)(h * DH) * DM * 2;
      const unsigned ve = (unsigned)((32 * aw + (lane >> 3)) * (DM * 2) + 1024 + 16 * (lane & 7));
#pragma unroll
      for (int i = 0; i < 4; ++i) { AWAIT(i); wv[32 + i] = LDO(wp + (size_t)(8 * i) * (DM * 2) + ve); }
      const unsigned vo = (unsigned)(32 * r * (DM * 2) + 256 * aw + 16 * c);
#pragma unroll
      for (int i = 0; i < 32; ++i) { AWAIT(i + 4); wv[i] = LDO(wp + (size_t)((((i >> 1) + ro) & 15) * 2 + (i & 1)) * (DM * 2) + vo); }
      SB();
#pragma unroll
      for (int i = 0; i < 4; ++i) *reinterpret_cast<v4u*>(ldsu + L_ST + (32 * aw + 8 * i) * 32 + 4 * lane) = wv[32 + i];
    }
    if (aw == 0) {
      const int sl = min(lane, 35);
      const unsigned tg = ldsu[L_TAG];
      const int off = WS_GU + (b * 2304 + fs * 72 + 2 * sl) * 8;
      v4u g, u;
#pragma unroll 1
      for (unsigned sp = 0; sp < SPIN_CAP / 16; ++sp) {
        asm volatile("" ::: "memory");
        g = __builtin_amdgcn_raw_buffer_load_b128(rs, off, 0, SC1);
        u = __builtin_amdgcn_raw_buffer_load_b128(rs, off + 8 * 2304 * 8, 0, SC1);
        if (__all(g[1] == tg && g[3] == tg && u[1] == tg && u[3] == tg)) break;
        __builtin_amdgcn_s_sleep(64);
      }
      float hh[4];
#pragma unroll
      for (int k = 0; k < 4; ++k) {
        const float gs = __uint_as_float((k & 1) ? g[k & 2] & 0xffff0000u : g[k & 2] << 16);
        const float us = __uint_as_float((k & 1) ? u[k & 2] & 0xffff0000u : u[k & 2] << 16);
        const float inner = 0.7978845608028654f * (gs + 0.044715f * gs * gs * gs);
        hh[k] = bf_round(0.5f * gs * (1.f + tanhf(inner))) * us;
      }
      *reinterpret_cast<v2u*>(ldsu + L_H + 2 * sl) = v2u{cvtpk(hh[0], hh[1]), cvtpk(hh[2], hh[3])};
    }
    bar += 4;
    aux_barrier(cnt, bar, lane);
#pragma unroll
    for (int i = 0; i < 4; ++i)
      wv[32 + i] = *reinterpret_cast<const v4u*>(ldsu + L_ST + (2 * (rl ^ (4 * (i & 2))) + (i & 1)) * 32 + 8 * aw + 4 * (c >> 3));
#pragma unroll
    for (int i = 0; i < 18; ++i) {
#pragma unroll
      for (int t = 0; t < 4; ++t) {
        const unsigned w0 = wv[2 * i][t], w1 = wv[2 * i + 1][t];
        wv[2 * i][t] = __builtin_amdgcn_perm(w1, w0, 0x05040100u);
        wv[2 * i + 1][t] = __builtin_amdgcn_perm(w1, w0, 0x07060302u);
      }
      asm volatile("" : "+v"(wv[2 * i]), "+v"(wv[2 * i + 1]));
      SB();
    }
    float gate;
    {
      float z = 0.f;
#pragma unroll
      for (int i = 0; i < 5; ++i) {
        const int dw = lane + 64 * i;
        if (dw < 288) z = dot2(reinterpret_cast<const unsigned*>(xb)[dw], reinterpret_cast<const unsigned*>(gw)[dw], z);
      }
      z = wave_sum(z);
      gate = 1.f / (1.f + __expf(-z));
    }
    if (ta < 216) {
      const int rg = ta / 72, cg = ta - rg * 72;
      float accd[8] = {0.f, 0.f, 0.f, 0.f, 0.f, 0.f, 0.f, 0.f};
      const char* wp = wd + (size_t)(fs * 144) * DM * 2;
      const unsigned vo = (unsigned)(48 * rg * DM + 8 * cg) * 2u;
      int pi = ROTD * (2 * rk % 24);
      v4u w[4];
      unsigned hv[2];
#pragma unroll
      for (int i = 0; i < 25; ++i) {
        if (i < 24) {
          hv[i & 1] = ldsu[L_H + 24 * rg + pi];
          AWAIT(2 * i); w[2 * (i & 1)] = LDD(wp + (size_t)pi * (DM * 4) + vo);
          AWAIT(2 * i + 1); w[2 * (i & 1) + 1] = LDD(wp + (size_t)pi * (DM * 4) + DM * 2 + vo);
          SB();
          pi = pi == 23 ? 0 : pi + 1;
        }
        if (i) gemv2(accd, w[2 * (~i & 1)], w[2 * (~i & 1) + 1], hv[~i & 1]);
        SB();
      }
      float* dst = lds + L_RED + rg * 576 + 8 * cg;
      *reinterpret_cast<v4f*>(dst) = v4f{accd[0], accd[1], accd[2], accd[3]};
      *reinterpret_cast<v4f*>(dst + 4) = v4f{accd[4], accd[5], accd[6], accd[7]};
    }
    bar += 4;
    aux_barrier(cnt, bar, lane);
    v2f dm, de;
    {
      const float* p = lds + L_RED + 128 * aw + 8 * c + 2 * r;
      const float* q = lds + L_RED + 512 + 16 * aw + (c & 8) + 2 * r;
      dm = (*reinterpret_cast<const v2f*>(p) + *reinterpret_cast<const v2f*>(p + 576) + *reinterpret_cast<const v2f*>(p + 1152)) * (1.f - gate);
      de = (*reinterpret_cast<const v2f*>(q) + *reinterpret_cast<const v2f*>(q + 576) + *reinterpret_cast<const v2f*>(q + 1152)) * (1.f - gate);
    }
    unsigned tlo = ldsu[L_MISC + 4], thi = ldsu[L_MISC + 5];
    unsigned om = WS_INBOX + ((b * 36 + 8 * aw + (c >> 1)) * 32 + h) * 128 + ((c & 1) * 4 + r) * 16;
    unsigned oe = (lane & 7) ? OOB : WS_INBOX + ((b * 36 + 32 + aw) * 32 + h) * 128 + ((c >> 3) * 4 + r) * 16;
    const float* mrg = lds + L_MRG + 2 * rl;
    asm volatile("" : "+v"(tlo), "+v"(thi), "+v"(om), "+v"(oe), "+v"(dm), "+v"(de), "+v"(gate));
#pragma unroll 1
    for (unsigned sp = 0; sp < SPIN_CAP; ++sp) {
      if (__builtin_amdgcn_readfirstlane(__hip_atomic_load(cnt + 6, __ATOMIC_RELAXED, __HIP_MEMORY_SCOPE_WORKGROUP)) >= 4u) break;
      __builtin_amdgcn_s_sleep(4);
    }
    asm volatile("" ::: "memory");
    float f[4];
    {
      const v4f a0 = *reinterpret_cast<const v4f*>(lds + L_ML), a1 = *reinterpret_cast<const v4f*>(lds + L_ML + 4);
      const float M = fmaxf(fmaxf(a0[0], a0[2]), fmaxf(a1[0], a1[2]));
      f[0] = __builtin_amdgcn_exp2f(a0[0] - M); f[1] = __builtin_amdgcn_exp2f(a0[2] - M);
      f[2] = __builtin_amdgcn_exp2f(a1[0] - M); f[3] = __builtin_amdgcn_exp2f(a1[2] - M);
      const float inv = 1.f / (f[0] * a0[1] + f[1] * a0[3] + f[2] * a1[1] + f[3] * a1[3]);
#pragma unroll
      for (int w = 0; w < 4; ++w) f[w] *= inv;
      asm volatile("" : "+v"(f[0]), "+v"(f[1]), "+v"(f[2]), "+v"(f[3]));
    }
    wg_barrier();
    {
      v2f o = {0.f, 0.f};
#pragma unroll
      for (int w = 0; w < 4; ++w) o += *reinterpret_cast<const v2f*>(mrg + w * 128) * f[w];
      const float op = __uint_as_float(cvtpk(o[0], o[1]));
      float am[8] = {0.f, 0.f, 0.f, 0.f, 0.f, 0.f, 0.f, 0.f}, ae[8] = {0.f, 0.f, 0.f, 0.f, 0.f, 0.f, 0.f, 0.f};
#define WO_STEP(A, I, OP)                                                                                   \
  { const unsigned o_ = __float_as_uint(OP);                                                                \
    _Pragma("unroll") for (int t = 0; t < 4; ++t) {                                                         \
      A[2 * t] = dot2(o_, wv[2 * (I)][t], A[2 * t]); A[2 * t + 1] = dot2(o_, wv[2 * (I) + 1][t], A[2 * t + 1]); } }
      WO_STEP(ae, 16, op) WO_STEP(ae, 17, dpp<XOR8>(op))
      v2f e = fold8(ae);
      WO_STEP(am, 0, dpp<BCAST + 0>(op)) WO_STEP(am, 1, dpp<BCAST + 1>(op)) WO_STEP(am, 2, dpp<BCAST + 2>(op)) WO_STEP(am, 3, dpp<BCAST + 3>(op))
      e[0] += dpp<XOR1>(e[0]); e[1] += dpp<XOR1>(e[1]);
      WO_STEP(am, 4, dpp<BCAST + 4>(op)) WO_STEP(am, 5, dpp<BCAST + 5>(op)) WO_STEP(am, 6, dpp<BCAST + 6>(op)) WO_STEP(am, 7, dpp<BCAST + 7>(op))
      e[0] += dpp<XOR2>(e[0]); e[1] += dpp<XOR2>(e[1]);
      WO_STEP(am, 8, dpp<BCAST + 8>(op)) WO_STEP(am, 9, dpp<BCAST + 9>(op)) WO_STEP(am, 10, dpp<BCAST + 10>(op)) WO_STEP(am, 11, dpp<BCAST + 11>(op))
      e[0] += dpp<XOR7>(e[0]); e[1] += dpp<XOR7>(e[1]);
      e = e * gate + de;
      __builtin_amdgcn_raw_buffer_store_b128(v4u{(__float_as_uint(e[0]) >> 8) | tlo, thi, (__float_as_uint(e[1]) >> 8) | tlo, thi}, rs, oe, 0, SC1);
      WO_STEP(am, 12, dpp<BCAST + 12>(op)) WO_STEP(am, 13, dpp<BCAST + 13>(op)) WO_STEP(am, 14, dpp<BCAST + 14>(op)) WO_STEP(am, 15, dpp<BCAST + 15>(op))
#undef WO_STEP
      const v2f v = fold8(am) * gate + dm;
      __builtin_amdgcn_raw_buffer_store_b128(v4u{(__float_as_uint(v[0]) >> 8) | tlo, thi, (__float_as_uint(v[1]) >> 8) | tlo, thi}, rs, om, 0, SC1);
    }
  }
}

extern "C" void kernel_launch(void* const* d_in, const int* in_sizes, int n_in, void* d_out, int out_size, void* d_ws,
                              size_t ws_size, hipStream_t stream) {
  (void)in_sizes; (void)n_in; (void)out_size;
  if (ws_size < (size_t)WS_TOTAL) return;
  fused_decode<<<dim3(NB * NH), dim3(512), 0, stream>>>(
      (const char*)d_in[0], (const char*)d_in[1], (const char*)d_in[2], (const char*)d_in[3], (const char*)d_in[4],
      (const char*)d_in[6], (const char*)d_in[7], (const char*)d_in[8], (const char*)d_in[9], (unsigned*)d_out,
      (char*)d_ws);
}
```

```cpp
#include <hip/hip_runtime.h>
#include <hip/hip_bf16.h>
#include <stdint.h>

typedef unsigned v4u __attribute__((ext_vector_type(4)));
typedef unsigned v2u __attribute__((ext_vector_type(2)));
typedef float v4f __attribute__((ext_vector_type(4)));
typedef float v2f __attribute__((ext_vector_type(2)));
typedef __bf16 bf16x2 __attribute__((ext_vector_type(2)));
typedef unsigned long long u64;

#define RLX_AGENT __ATOMIC_RELAXED, __HIP_MEMORY_SCOPE_AGENT

constexpr int NB = 8, NH = 32, SK = 4096, DH = 128, DM = 576, DF = 4608;
constexpr size_t WS_EPOCH = 0;
constexpr int WS_INBOX = 12288;
constexpr int WS_GU = WS_INBOX + 8 * 36 * 32 * 64;
constexpr int WS_QIN = WS_GU + 2 * 8 * 2304 * 8;
constexpr int WS_MARK = WS_QIN + 256 * 2048;
constexpr int WS_TOTAL = WS_MARK + 8 * 64 * 32;
constexpr unsigned SPIN_CAP = 1u << 18;

constexpr int L_SC = 0;
constexpr int L_QP = L_SC + 4096;
constexpr int L_GU = L_QP + 512;
constexpr int L_H = L_GU + 4096;
constexpr int L_MRG = L_H + 72;
constexpr int L_ML = L_MRG + 512;
constexpr int L_RED = L_ML + 8;
constexpr int L_MISC = L_RED + 3 * 576;
constexpr int L_TAG = L_MISC + 8;
constexpr int L_CTL = L_TAG + 8;
constexpr int L_ST = L_CTL + 8;
constexpr int L_TOTAL = L_ST + 4096;

__device__ __forceinline__ float dot2(unsigned a, unsigned b, float acc) {
  return __builtin_amdgcn_fdot2_f32_bf16(__builtin_bit_cast(bf16x2, a), __builtin_bit_cast(bf16x2, b), acc, false);
}
__device__ __forceinline__ unsigned cvtpk(float lo, float hi) {
  unsigned r; asm("v_cvt_pk_bf16_f32 %0, %1, %2" : "=v"(r) : "v"(lo), "v"(hi)); return r;
}
__device__ __forceinline__ float bf_round(float x) { return __builtin_bit_cast(float, cvtpk(x, x) << 16); }
template <int CTRL> __device__ __forceinline__ float dpp(float x) {
  return __builtin_bit_cast(float, __builtin_amdgcn_mov_dpp(__builtin_bit_cast(int, x), CTRL, 0xf, 0xf, true));
}
constexpr int XOR1 = 0xB1, XOR2 = 0x4E, XOR7 = 0x141, XOR8 = 0x128, BCAST = 0x150;
__device__ __forceinline__ float xrow_sum(float x) {
  auto s = __builtin_amdgcn_permlane16_swap(__float_as_uint(x), __float_as_uint(x), false, false);
  x = __uint_as_float(s[0]) + __uint_as_float(s[1]);
  auto t = __builtin_amdgcn_permlane32_swap(__float_as_uint(x), __float_as_uint(x), false, false);
  return __uint_as_float(t[0]) + __uint_as_float(t[1]);
}
__device__ __forceinline__ float xrow_max(float x) {
  auto s = __builtin_amdgcn_permlane16_swap(__float_as_uint(x), __float_as_uint(x), false, false);
  x = fmaxf(__uint_as_float(s[0]), __uint_as_float(s[1]));
  auto t = __builtin_amdgcn_permlane32_swap(__float_as_uint(x), __float_as_uint(x), false, false);
  return fmaxf(__uint_as_float(t[0]), __uint_as_float(t[1]));
}
__device__ __forceinline__ v2f fold8(const float (&a)[8]) {
  float x[4];
#pragma unroll
  for (int k = 0; k < 4; ++k) {
    auto s = __builtin_amdgcn_permlane32_swap(__float_as_uint(a[k]), __float_as_uint(a[k + 4]), false, false);
    x[k] = __uint_as_float(s[0]) + __uint_as_float(s[1]);
  }
  v2f y;
#pragma unroll
  for (int k = 0; k < 2; ++k) {
    auto s = __builtin_amdgcn_permlane16_swap(__float_as_uint(x[k]), __float_as_uint(x[k + 2]), false, false);
    y[k] = __uint_as_float(s[0]) + __uint_as_float(s[1]);
  }
  return y;
}
__device__ __forceinline__ float wave_sum(float x) {
  x += dpp<XOR1>(x); x += dpp<XOR2>(x); x += dpp<XOR7>(x); x += dpp<XOR8>(x);
  return xrow_sum(x);
}
__device__ __forceinline__ float wave_max(float x) {
  x = fmaxf(x, dpp<XOR1>(x)); x = fmaxf(x, dpp<XOR2>(x)); x = fmaxf(x, dpp<XOR7>(x)); x = fmaxf(x, dpp<XOR8>(x));
  return xrow_max(x);
}
__device__ __forceinline__ v4u ld_nt(const char* p) { return __builtin_nontemporal_load(reinterpret_cast<const v4u*>(p)); }
__device__ __forceinline__ v4u ld_w(const char* p) { return *reinterpret_cast<const v4u*>(p); }
__device__ __forceinline__ void wg_barrier() {
  asm volatile("s_waitcnt lgkmcnt(0)" ::: "memory");
  __builtin_amdgcn_s_barrier();
  asm volatile("" ::: "memory");
}
#define SB() __builtin_amdgcn_sched_barrier(0)
#define PK 5
#define PA 2
#define PAG 2
#define GPAT 1
#define QPAT 0
#define QPACE 63
#define LDQ ld_nt
#define LDG ld_nt
#define LD5 ld_nt
#define LDO ld_w
#define LDD ld_w
#define BURST 5
#define ROTD 1
#define ROTO 1
#define TS0 20
#define QS0 32
#define QS1 8
constexpr unsigned OOB = 0x80000000u;
constexpr int PACE_S = PK, PACE_A = PA;
#define PWAIT(N) asm volatile("s_waitcnt vmcnt(%0)" ::"n"((N) - 1) : "memory")
#define DWAIT(OPEN) asm volatile("s_cmp_gt_i32 %0, 0\n\ts_cbranch_scc1 1f\n\ts_waitcnt vmcnt(%1)\n1:" ::"s"(OPEN), "n"(PACE_S - 1) : "memory", "scc")
constexpr int MK0 = 40, MKS = 3, XC = 7, NM = 16, FROM = 208;
constexpr int RX0 = 64, RX1 = 120, RX2 = 176, DK0 = 28, DK1 = 24, DK2 = 16;
constexpr int J0 = (RX0 - MK0) / MKS - 5, J1 = (RX1 - MK0) / MKS - 5, J2 = (RX2 - MK0) / MKS - 5;
static_assert(NM == 16 && J0 >= 0 && J1 >= J0 + NM && J2 >= J1 + NM && J2 + NM <= 64 && MK0 + MKS * (J2 + NM) <= 238 && RX2 <= 238, "marks");
#define AWAIT(I) do { SB(); if ((I) >= PACE_A) PWAIT(PACE_A); SB(); } while (0)

__device__ __forceinline__ void gemv2(float (&acc)[8], v4u w0, v4u w1, unsigned xp) {
#pragma unroll
  for (int t = 0; t < 4; ++t) {
    acc[2 * t] = dot2(__builtin_amdgcn_perm(w1[t], w0[t], 0x05040100u), xp, acc[2 * t]);
    acc[2 * t + 1] = dot2(__builtin_amdgcn_perm(w1[t], w0[t], 0x07060302u), xp, acc[2 * t + 1]);
  }
}

typedef __bf16 bf16x8 __attribute__((ext_vector_type(8)));
__device__ __forceinline__ v4f mfma32(v4u a, v4u b, v4f c) {
  return __builtin_amdgcn_mfma_f32_16x16x32_bf16(__builtin_bit_cast(bf16x8, a), __builtin_bit_cast(bf16x8, b), c, 0, 0, 0);
}
__device__ __forceinline__ unsigned pair(unsigned row1, unsigned row0, int hi) {
  return __builtin_amdgcn_perm(row1, row0, hi ? 0x07060302u : 0x05040100u);
}
__device__ __forceinline__ u64 mix64(u64 e) {
  u64 z = e * 0x9E3779B97F4A7C15ull;
  z = (z ^ (z >> 30)) * 0xBF58476D1CE4E5B9ull;
  z = (z ^ (z >> 27)) * 0x94D049BB133111EBull;
  return z ^ (z >> 31);
}
constexpr int SC1 = 16;

#define UNIT_LOADS(LD, WP, XA, STRIDE, PACE, PAT, SLOW)                                                     \
  v4u x0, x1;                                                                                               \
  unsigned x2;                                                                                              \
  if ((PAT) == 2) {                                                                                         \
    _Pragma("unroll") for (int j = 0; j < 4; ++j) {                                                         \
      x0[j] = *reinterpret_cast<const unsigned*>((XA) + 4 * ra + 32 * j);                                   \
      x1[j] = *reinterpret_cast<const unsigned*>((XA) + 128 + 4 * ra + 32 * j);                             \
    }                                                                                                       \
    x2 = *reinterpret_cast<const unsigned*>((XA) + 256 + 4 * ra);                                           \
  } else if ((PAT) == 3) {                                                                                  \
    const unsigned short* xs_ = reinterpret_cast<const unsigned short*>((XA) + 2 * ra);                     \
    _Pragma("unroll") for (int j = 0; j < 4; ++j) {                                                         \
      x0[j] = xs_[16 * j] | (unsigned)xs_[16 * j + 8] << 16;                                                \
      x1[j] = xs_[64 + 16 * j] | (unsigned)xs_[64 + 16 * j + 8] << 16;                                      \
    }                                                                                                       \
    x2 = xs_[128] | (unsigned)xs_[136] << 16;                                                               \
  } else {                                                                                                  \
    x0 = *reinterpret_cast<const v4u*>((XA) + 16 * ra); x1 = *reinterpret_cast<const v4u*>((XA) + 128 + 16 * ra); \
    x2 = *reinterpret_cast<const unsigned*>((XA) + 256 + 4 * ra);                                           \
  }                                                                                                         \
  v4u w[18];                                                                                                \
  {                                                                                                         \
    const unsigned vo8 = (unsigned)(8 * (lane >> 3) * (STRIDE) + 16 * (lane & 7));                          \
    const unsigned vo2 = (unsigned)(2 * (lane >> 3) * (STRIDE) + 16 * (lane & 7));                          \
    const unsigned vo1 = (unsigned)((lane >> 3) * (STRIDE) + 16 * (lane & 7));                              \
    _Pragma("unroll") for (int i = 0; i < 18; ++i) {                                                        \
      SB(); if ((PACE) < 32) PWAIT(PACE);                                                                   \
      if ((PAT) == 1) asm volatile("s_cmp_lg_u32 %0, 3\n\ts_cbranch_scc1 1f\n\ts_waitcnt vmcnt(0)\n1:" ::"s"(SLOW) : "memory", "scc"); \
      SB();                                                                                                 \
      w[i] = LD((WP) + (size_t)((PAT) == 2 ? 16 * (i >> 1) + (i & 1) : (PAT) == 3 ? 8 * i : i < 16 ? (i >> 3) * 64 + (i & 7) : 112 + i) * (STRIDE) + \
                ((PAT) == 3 ? vo1 : (PAT) == 2 || i >= 16 ? vo2 : vo8));                                    \
    }                                                                                                       \
    SB();                                                                                                   \
  }
#define UNIT_MFMA()                                                                                         \
  v4f acc[8];                                                                                               \
  _Pragma("unroll") for (int t = 0; t < 8; ++t) {                                                           \
    v4f d = {0.f, 0.f, 0.f, 0.f};                                                                           \
    _Pragma("unroll") for (int sI = 0; sI < 2; ++sI) {                                                      \
      v4u f;                                                                                                \
      _Pragma("unroll") for (int k = 0; k < 4; ++k) f[k] = pair(w[8 * sI + 2 * k + 1][t >> 1], w[8 * sI + 2 * k][t >> 1], t & 1); \
      d = mfma32(sI ? x1 : x0, f, d);                                                                       \
    }                                                                                                       \
    acc[t] = mfma32(v4u{x2, 0u, 0u, 0u}, v4u{pair(w[17][t >> 1], w[16][t >> 1], t & 1), 0u, 0u, 0u}, d);    \
  }

typedef __attribute__((address_space(3))) unsigned lds_u32;
__device__ __forceinline__ void aux_barrier(lds_u32* cnt, unsigned target, int lane) {
  asm volatile("s_waitcnt lgkmcnt(0)" ::: "memory");
  if (lane == 0) __hip_atomic_fetch_add(cnt, 1u, __ATOMIC_RELAXED, __HIP_MEMORY_SCOPE_WORKGROUP);
#pragma unroll 1
  for (unsigned s = 0; s < SPIN_CAP; ++s) {
    const unsigned v = __builtin_amdgcn_readfirstlane(__hip_atomic_load(cnt, __ATOMIC_RELAXED, __HIP_MEMORY_SCOPE_WORKGROUP));
    if (v >= target) break;
    __builtin_amdgcn_s_sleep(2);
  }
  asm volatile("" ::: "memory");
}

__device__ __forceinline__ void lds_wait(lds_u32* p, unsigned target) {
#pragma unroll 1
  for (unsigned s = 0; s < SPIN_CAP; ++s) {
    if (__builtin_amdgcn_readfirstlane(__hip_atomic_load(p, __ATOMIC_RELAXED, __HIP_MEMORY_SCOPE_WORKGROUP)) >= target) break;
    __builtin_amdgcn_s_sleep(4);
  }
  asm volatile("" ::: "memory");
}

__global__ void __launch_bounds__(512, 2)
fused_decode(const char* __restrict__ xg, const char* kc, const char* vc,
             const char* __restrict__ wq, const char* __restrict__ wo, const char* __restrict__ wg,
             const char* __restrict__ wu, const char* __restrict__ wd, const char* __restrict__ gw,
             unsigned* __restrict__ out, char* __restrict__ ws) {
  __shared__ __attribute__((aligned(16))) float lds[L_TOTAL];
  unsigned* ldsu = reinterpret_cast<unsigned*>(lds);
  const int tid = threadIdx.x ^ 256, lane = tid & 63;
  const int wave = __builtin_amdgcn_readfirstlane(tid >> 6);
  const int bh = blockIdx.x, b = bh >> 5, h = bh & 31;
  const int r = lane >> 4, c = lane & 15;

  if (wave < 4) {
    const char* kb = kc + (size_t)bh * (SK * DH * 2) + wave * 1024;
    const char* vb = vc + (size_t)bh * (SK * DH * 2) + wave * 1024;
    const unsigned voff = lane * 16;
    v4u a[32];
#pragma unroll
    for (int j = 0; j < BURST; ++j) { a[j] = LD5(kb + j * 4096 + voff); asm volatile("" ::: "memory"); SB(); }
#pragma unroll
    for (int j = BURST; j < 32; ++j) { PWAIT(PACE_S); a[j] = ld_nt(kb + j * 4096 + voff); asm volatile("" ::: "memory"); SB(); }
    wg_barrier();
    const v4u q = *reinterpret_cast<const v4u*>(ldsu + L_QP + 4 * c);
    constexpr float C2 = 0.08838834764831845f * 1.4426950408889634f;
    float* sc = lds + L_SC + wave * 1024 + lane;
    float m = -INFINITY;
#define TR_STEP(HALF, CTRL, BIT)                                                                           \
  _Pragma("unroll") for (int u = 0; u < (HALF); ++u) {                                                      \
    const float lo_ = d[u], hi_ = d[u + (HALF)];                                                            \
    const float t1_ = lo_ + dpp<CTRL>(lo_), t2_ = hi_ + dpp<CTRL>(hi_);                                     \
    d[u] = (c & (BIT)) ? t2_ : t1_;                                                                         \
  }
#define QK_GROUP(GIDX, SLOT0, RELOAD, PFB)                                                                  \
  {                                                                                                         \
    float d[16];                                                                                            \
    _Pragma("unroll") for (int j = 0; j < 16; ++j) {                                                        \
      const v4u t = a[(SLOT0) + j];                                                                         \
      float s = dot2(t[0], q[0], 0.f);                                                                      \
      s = dot2(t[1], q[1], s); s = dot2(t[2], q[2], s); s = dot2(t[3], q[3], s);                            \
      asm volatile("" : "+v"(s)::"memory");                                                                 \
      d[j] = s;                                                                                             \
      SB();                                                                                                 \
      if (RELOAD) { PWAIT(PACE_S); a[(SLOT0) + j] = ld_nt((PFB) + j * 4096 + voff); }                       \
      asm volatile("" ::: "memory");                                                                        \
      SB();                                                                                                 \
    }                                                                                                       \
    TR_STEP(8, XOR8, 8) TR_STEP(4, XOR7, 4) TR_STEP(2, XOR2, 2) TR_STEP(1, XOR1, 1)                         \
    const float s = d[0] * C2;                                                                              \
    m = fmaxf(m, s);                                                                                        \
    sc[(GIDX) * 64] = s;                                                                                    \
  }
    QK_GROUP(0, 0, true, kb + 2 * 65536)
    QK_GROUP(1, 16, false, kb)
    for (int it = 2; it < 16; ++it) {
      const char* pf = (it + 1 < 16) ? kb + (size_t)(it + 1) * 65536 : vb;
      QK_GROUP(it, 0, true, pf)
    }
#undef TR_STEP
#undef QK_GROUP
    m = wave_max(m);
    float acc[8] = {0.f, 0.f, 0.f, 0.f, 0.f, 0.f, 0.f, 0.f};
    float pn = __builtin_amdgcn_exp2f(sc[0] - m), l = pn;
#define PV_GROUP(GIDX, SLOT0, RELOAD, PFB)                                                                  \
  {                                                                                                         \
    const float p = pn;                                                                                     \
    if (RELOAD) { pn = __builtin_amdgcn_exp2f(sc[((GIDX) + 1) * 64] - m); l += pn; }                        \
    float pj;                                                                                               \
    PV_TILE(0, SLOT0, RELOAD, PFB) PV_TILE(1, SLOT0, RELOAD, PFB) PV_TILE(2, SLOT0, RELOAD, PFB)            \
    PV_TILE(3, SLOT0, RELOAD, PFB) PV_TILE(4, SLOT0, RELOAD, PFB) PV_TILE(5, SLOT0, RELOAD, PFB)            \
    PV_TILE(6, SLOT0, RELOAD, PFB) PV_TILE(7, SLOT0, RELOAD, PFB) PV_TILE(8, SLOT0, RELOAD, PFB)            \
    PV_TILE(9, SLOT0, RELOAD, PFB) PV_TILE(10, SLOT0, RELOAD, PFB) PV_TILE(11, SLOT0, RELOAD, PFB)          \
    PV_TILE(12, SLOT0, RELOAD, PFB) PV_TILE(13, SLOT0, RELOAD, PFB) PV_TILE(14, SLOT0, RELOAD, PFB)         \
    PV_TILE(15, SLOT0, RELOAD, PFB)                                                                         \
  }
#define PV_TILE(J, SLOT0, RELOAD, PFB)                                                                      \
  {                                                                                                         \
    const v4u t = a[(SLOT0) + (J)];                                                                         \
    pj = dpp<BCAST + (J)>(p);                                                                               \
    _Pragma("unroll") for (int k = 0; k < 4; ++k) {                                                         \
      acc[2 * k] = fmaf(pj, __builtin_bit_cast(float, t[k] << 16), acc[2 * k]);                             \
      acc[2 * k + 1] = fmaf(pj, __builtin_bit_cast(float, t[k] & 0xffff0000u), acc[2 * k + 1]);             \
    }                                                                                                       \
    asm volatile("" : "+v"(acc[0]), "+v"(acc[1]), "+v"(acc[2]), "+v"(acc[3]), "+v"(acc[4]), "+v"(acc[5]),   \
                 "+v"(acc[6]), "+v"(acc[7])::"memory");                                                     \
    SB();                                                                                                   \
    if (RELOAD) {                                                                                           \
      if (((J) & 3) == 1) {                                                                                 \
        const int cw = __builtin_amdgcn_readfirstlane(win);                                                 \
        open = max((cw & 0xFFFF) - (pos + (J)), pos + (J) + 1 - (cw >> 16));                                \
      }                                                                                                     \
      DWAIT(open); a[(SLOT0) + (J)] = ld_nt((PFB) + (J) * 4096 + voff);                                     \
      if (((J) & 1) == 0) __hip_atomic_store(ctl, (unsigned)(pos + (J)), __ATOMIC_RELAXED, __HIP_MEMORY_SCOPE_WORKGROUP); \
      if (((J) & 3) == 0) win = __hip_atomic_load(ctl + 4, __ATOMIC_RELAXED, __HIP_MEMORY_SCOPE_WORKGROUP);   \
    }                                                                                                       \
    asm volatile("" ::: "memory");                                                                          \
    SB();                                                                                                   \
  }
    lds_u32* ctl = (lds_u32*)(ldsu + L_CTL + wave);
    int open = 0;
    unsigned win = 0u;
    for (int it = 0; it < 15; ++it) {
      const char* pf = vb + (size_t)(it + 1) * 65536;
      const int pos = 16 * it;
      PV_GROUP(it, 0, true, pf)
    }
    l = wave_sum(l);
    if (lane == 0) {
      *reinterpret_cast<v2f*>(lds + L_ML + 2 * wave) = v2f{m, l};
      asm volatile("s_waitcnt lgkmcnt(0)" ::: "memory");
      __hip_atomic_fetch_add((lds_u32*)(ldsu + L_MISC + 6), 1u, __ATOMIC_RELAXED, __HIP_MEMORY_SCOPE_WORKGROUP);
    }
    const unsigned t0 = ldsu[L_MISC + 4], t1 = ldsu[L_MISC + 5] << 24, t2 = ldsu[L_MISC + 7], t3 = ldsu[L_MISC + 5] << 16;
    { const int pos = 0; PV_GROUP(15, 0, false, vb) }
#undef PV_GROUP
#undef PV_TILE
    *reinterpret_cast<v2f*>(lds + L_MRG + wave * 128 + 8 * c + 2 * r) = fold8(acc);
    wg_barrier();
    if (wave == 0 || (wave == 1 && h < 4)) {
      const __amdgpu_buffer_rsrc_t rs = __builtin_amdgcn_make_buffer_rsrc(ws, 0, WS_TOTAL, 0x00020000);
      const int blk = 32 * wave + h;
      unsigned off[2];
      v4u va[2];
#pragma unroll
      for (int p = 0; p < 2; ++p) { off[p] = WS_INBOX + (b * 36 + blk) * 2048 + p * 1024 + lane * 16; va[p] = v4u{0u, 0u, 0u, 0u}; }
      __builtin_amdgcn_s_sleep(TS0);
#pragma unroll 1
      for (unsigned sp = 0; sp < SPIN_CAP; ++sp) {
        asm volatile("" ::: "memory");
        v4u g[2];
#pragma unroll
        for (int p = 0; p < 2; ++p) g[p] = __builtin_amdgcn_raw_buffer_load_b128(rs, off[p], 0, SC1);
#pragma unroll
        for (int p = 0; p < 2; ++p) {
          const bool ok = ((g[p][0] ^ t0) | (g[p][1] ^ t1) | (g[p][2] ^ t2) | (g[p][3] ^ t3)) < (1u << 24);
          va[p] = ok ? g[p] : va[p]; off[p] = ok ? OOB : off[p];
        }
        if (__all((off[0] & off[1]) == OOB)) break;
      }
      float sv[4];
#pragma unroll
      for (int k = 0; k < 4; ++k) {
        sv[k] = __uint_as_float(va[0][k] << 8) + __uint_as_float(va[1][k] << 8);
        sv[k] += dpp<0x124>(sv[k]); sv[k] += dpp<XOR8>(sv[k]);
      }
#pragma unroll
      for (int k = 0; k < 2; ++k) {
        auto x = __builtin_amdgcn_permlane32_swap(__float_as_uint(sv[k]), __float_as_uint(sv[k + 2]), false, false);
        sv[k] = __uint_as_float(x[0]) + __uint_as_float(x[1]);
        auto y = __builtin_amdgcn_permlane16_swap(__float_as_uint(sv[k]), __float_as_uint(sv[k]), false, false);
        sv[k] = __uint_as_float(y[0]) + __uint_as_float(y[1]);
      }
      if ((lane & 28) == 0) out[b * (DM / 2) + blk * 8 + 4 * (lane & 1) + (lane & 2) + (lane >> 5)] = cvtpk(sv[0], sv[1]);
      if (tid == 0 && bh == 0)
        __hip_atomic_store(reinterpret_cast<u64*>(ws + WS_EPOCH), (((u64)ldsu[L_MISC + 3] << 32) | ldsu[L_MISC + 2]) + 1ull, RLX_AGENT);
    }
  } else {
    const int aw = wave - 4, ta = tid - 256;
    const int fs = (h & 7) * 4 + (h >> 3);
    const char* xb = xg + b * (DM * 2);
    const __amdgpu_buffer_rsrc_t rs = __builtin_amdgcn_make_buffer_rsrc(ws, 0, WS_TOTAL, 0x00020000);
    const int ra = 2 * r + (c >> 3);
    if (aw == 0) {
      const u64 ep = __hip_atomic_load(reinterpret_cast<u64*>(ws + WS_EPOCH), RLX_AGENT);
      UNIT_LOADS(LDQ, wq + (size_t)(144 * (b >> 1)) * (NH * DH * 2) + h * 256 + (b & 1) * 128, xg + (c & 7) * (DM * 2) + 288 * (b >> 1), NH * DH * 2, QPACE, QPAT, 0)
      const unsigned elo = __builtin_amdgcn_readfirstlane((unsigned)ep), ehi = __builtin_amdgcn_readfirstlane((unsigned)(ep >> 32));
      const u64 p1 = (((u64)ehi << 32) | elo) + 1ull;
      const u64 mx = mix64(p1);
      const unsigned thi = (unsigned)(mx >> 32) | 1u, tlo = (unsigned)p1 << 24;
      const unsigned t1 = thi << 24, t2 = tlo + ((unsigned)(((p1 >> 8) * 0x9E3779B97F4A7C15ull) >> 32) & 0xFF000000u), t3 = thi << 16 & 0xFF000000u;
      SB();
      UNIT_MFMA()
#pragma unroll
      for (int jj = 0; jj < 2; ++jj) {
        float sv[8];
#pragma unroll
        for (int t = 0; t < 8; ++t) {
          auto sw = __builtin_amdgcn_permlane32_swap(__float_as_uint(acc[t][jj]), __float_as_uint(acc[t][2 + jj]), false, false);
          sv[t] = __uint_as_float(sw[0]) + dpp<XOR8>(__uint_as_float(sw[1]));
        }
#pragma unroll
        for (int t = 0; t < 4; ++t) { const float o = dpp<XOR8>(sv[t + 4]); sv[t] = c < 8 ? sv[t] : o; }
        const int cb = 4 * (r & 1) + 2 * (r >> 1) + jj;
        __builtin_amdgcn_raw_buffer_store_b128(
            v4u{(__float_as_uint(sv[0]) >> 8) | tlo, (__float_as_uint(sv[1]) >> 8) | t1, (__float_as_uint(sv[2]) >> 8) | t2, (__float_as_uint(sv[3]) >> 8) | t3},
            rs, WS_QIN + (cb * 32 + h) * 2048 + b * 256 + c * 16, 0, SC1);
      }
      if (lane == 0) {
        ldsu[L_MISC + 0] = 0u;
        ldsu[L_MISC + 2] = elo; ldsu[L_MISC + 3] = ehi;
        ldsu[L_MISC + 4] = tlo; ldsu[L_MISC + 5] = thi; ldsu[L_MISC + 6] = 0u;
        ldsu[L_MISC + 7] = t2;
        ldsu[L_TAG] = ((unsigned)mx & ~0xFFu) | ((unsigned)p1 & 0xFFu);
      }
      v4u g[2];
      unsigned pend = 3u;
      __builtin_amdgcn_s_sleep(QS0);
#pragma unroll 1
      for (unsigned sp = 0; sp < SPIN_CAP; ++sp) {
        asm volatile("" ::: "memory");
#pragma unroll
        for (int p = 0; p < 2; ++p)
          if (pend >> p & 1) g[p] = __builtin_amdgcn_raw_buffer_load_b128(rs, WS_QIN + bh * 2048 + p * 1024 + lane * 16, 0, SC1);
#pragma unroll
        for (int p = 0; p < 2; ++p) {
          const unsigned bad = ((g[p][0] ^ tlo) | (g[p][1] ^ t1) | (g[p][2] ^ t2) | (g[p][3] ^ t3)) >> 24;
          if (__all(bad == 0)) pend &= ~(1u << p);
        }
        if (pend == 0) break;
        __builtin_amdgcn_s_sleep(QS1);
      }
      float qv[4];
#pragma unroll
      for (int k = 0; k < 4; ++k) qv[k] = __uint_as_float(g[0][k] << 8) + __uint_as_float(g[1][k] << 8);
#pragma unroll
      for (int k = 0; k < 2; ++k) {
        auto x = __builtin_amdgcn_permlane32_swap(__float_as_uint(qv[k]), __float_as_uint(qv[k + 2]), false, false);
        qv[k] = __uint_as_float(x[0]) + __uint_as_float(x[1]);
      }
      ldsu[L_QP + 32 * (r & 1) + 4 * (c & 7) + 2 * (c >> 3) + (r >> 1)] = cvtpk(qv[0], qv[1]);
    }
    if (aw == 1 && lane < 8) ldsu[L_CTL + lane] = lane < 4 ? 0u : FROM << 16;
    wg_barrier();
    lds_u32* cnt = (lds_u32*)(ldsu + L_MISC);
    unsigned bar = 0;
    if (bh < 144) {
      const int gu = (bh & 7) * 18 + (bh >> 3), mat = gu >= 72, jc = gu - 72 * mat;
      UNIT_LOADS(LDG, (mat ? wu : wg) + (size_t)(144 * aw) * (DF * 2) + jc * 128, xg + (c & 7) * (DM * 2) + 288 * aw, DF * 2, PAG, GPAT, (jc & 7))
      UNIT_MFMA()
      if ((r >> 1) == (c >> 3)) {
#pragma unroll
        for (int j = 0; j < 4; ++j) {
          float* dst = lds + L_GU + ((aw * 2 + (c >> 3)) * 8 + 4 * (r & 1) + j) * 64 + 8 * (c & 7);
          *reinterpret_cast<v4f*>(dst) = v4f{acc[0][j], acc[1][j], acc[2][j], acc[3][j]};
          *reinterpret_cast<v4f*>(dst + 4) = v4f{acc[4][j], acc[5][j], acc[6][j], acc[7][j]};
        }
      }
      bar += 4;
      aux_barrier(cnt, bar, lane);
      float s0 = 0.f, s1 = 0.f;
#pragma unroll
      for (int p = 0; p < 8; ++p) {
        const v2f v = *reinterpret_cast<const v2f*>(lds + L_GU + p * 512 + 2 * ta);
        s0 += v[0]; s1 += v[1];
      }
      const int pb = ta >> 5;
      __hip_atomic_store(reinterpret_cast<u64*>(ws + WS_GU) + (mat * 8 + pb) * 2304 + jc * 32 + (ta & 31),
                         ((u64)ldsu[L_TAG] << 32) | cvtpk(s0, s1), RLX_AGENT);
    }
    const int rk = (7 - b) * (h < 16 ? 4 : 3);
    const int ro = ROTO * (rk * 4 / 3);
    const int rl = 16 * r + ((c + ro) & 15);
    v4u wv[36];
    {
      const char* wp = wo + (size_t)(h * DH) * DM * 2;
      const unsigned ve = (unsigned)((32 * aw + (lane >> 3)) * (DM * 2) + 1024 + 16 * (lane & 7));
#pragma unroll
      for (int i = 0; i < 4; ++i) { AWAIT(i); wv[32 + i] = LDO(wp + (size_t)(8 * i) * (DM * 2) + ve); }
      const unsigned vo = (unsigned)(32 * r * (DM * 2) + 256 * aw + 16 * c);
#pragma unroll
      for (int i = 0; i < 32; ++i) { AWAIT(i + 4); wv[i] = LDO(wp + (size_t)((((i >> 1) + ro) & 15) * 2 + (i & 1)) * (DM * 2) + vo); }
      SB();
#pragma unroll
      for (int i = 0; i < 4; ++i) *reinterpret_cast<v4u*>(ldsu + L_ST + (32 * aw + 8 * i) * 32 + 4 * lane) = wv[32 + i];
    }
    if (aw == 0) {
      const int sl = min(lane, 35);
      const unsigned tg = ldsu[L_TAG];
      const int off = WS_GU + (b * 2304 + fs * 72 + 2 * sl) * 8;
      v4u g, u;
#pragma unroll 1
      for (unsigned sp = 0; sp < SPIN_CAP / 16; ++sp) {
        asm volatile("" ::: "memory");
        g = __builtin_amdgcn_raw_buffer_load_b128(rs, off, 0, SC1);
        u = __builtin_amdgcn_raw_buffer_load_b128(rs, off + 8 * 2304 * 8, 0, SC1);
        if (__all(g[1] == tg && g[3] == tg && u[1] == tg && u[3] == tg)) break;
        __builtin_amdgcn_s_sleep(64);
      }
      float hh[4];
#pragma unroll
      for (int k = 0; k < 4; ++k) {
        const float gs = __uint_as_float((k & 1) ? g[k & 2] & 0xffff0000u : g[k & 2] << 16);
        const float us = __uint_as_float((k & 1) ? u[k & 2] & 0xffff0000u : u[k & 2] << 16);
        const float inner = 0.7978845608028654f * (gs + 0.044715f * gs * gs * gs);
        hh[k] = bf_round(0.5f * gs * (1.f + tanhf(inner))) * us;
      }
      *reinterpret_cast<v2u*>(ldsu + L_H + 2 * sl) = v2u{cvtpk(hh[0], hh[1]), cvtpk(hh[2], hh[3])};
    }
    bar += 4;
    aux_barrier(cnt, bar, lane);
#pragma unroll
    for (int i = 0; i < 16; ++i) {
      const unsigned short* p = reinterpret_cast<const unsigned short*>(ldsu + L_ST) + (32 * r + 2 * ((i + ro) & 15)) * 64 + 16 * aw + c;
      wv[32 + (i >> 2)][i & 3] = p[0] | (unsigned)p[64] << 16;
    }
    asm volatile("" : "+v"(wv[32]), "+v"(wv[33]), "+v"(wv[34]), "+v"(wv[35]));
#pragma unroll
    for (int s = 0; s < 4; ++s) {
      v4u t[8];
#pragma unroll
      for (int k = 0; k < 8; ++k) t[k] = wv[8 * s + k];
#pragma unroll
      for (int j = 0; j < 8; ++j) {
#pragma unroll
        for (int u = 0; u < 4; ++u) wv[8 * s + j][u] = pair(t[2 * u + 1][j >> 1], t[2 * u][j >> 1], j & 1);
        asm volatile("" : "+v"(wv[8 * s + j]));
      }
      SB();
    }
    float gate;
    {
      float z = 0.f;
#pragma unroll
      for (int i = 0; i < 5; ++i) {
        const int dw = lane + 64 * i;
        if (dw < 288) z = dot2(reinterpret_cast<const unsigned*>(xb)[dw], reinterpret_cast<const unsigned*>(gw)[dw], z);
      }
      z = wave_sum(z);
      gate = 1.f / (1.f + __expf(-z));
    }
    if (ta < 216) {
      const int rg = ta / 72, cg = ta - rg * 72;
      float accd[8] = {0.f, 0.f, 0.f, 0.f, 0.f, 0.f, 0.f, 0.f};
      const char* wp = wd + (size_t)(fs * 144) * DM * 2;
      const unsigned vo = (unsigned)(48 * rg * DM + 8 * cg) * 2u;
      int pi = ROTD * (2 * rk % 24);
      v4u w[4];
      unsigned hv[2];
#pragma unroll
      for (int i = 0; i < 25; ++i) {
        if (i < 24) {
          hv[i & 1] = ldsu[L_H + 24 * rg + pi];
          AWAIT(2 * i); w[2 * (i & 1)] = LDD(wp + (size_t)pi * (DM * 4) + vo);
          AWAIT(2 * i + 1); w[2 * (i & 1) + 1] = LDD(wp + (size_t)pi * (DM * 4) + DM * 2 + vo);
          SB();
          pi = pi == 23 ? 0 : pi + 1;
        }
        if (i) gemv2(accd, w[2 * (~i & 1)], w[2 * (~i & 1) + 1], hv[~i & 1]);
        SB();
      }
      float* dst = lds + L_RED + rg * 576 + 8 * cg;
      *reinterpret_cast<v4f*>(dst) = v4f{accd[0], accd[1], accd[2], accd[3]};
      *reinterpret_cast<v4f*>(dst + 4) = v4f{accd[4], accd[5], accd[6], accd[7]};
    }
    bar += 4;
    aux_barrier(cnt, bar, lane);
    v4f dm;
    {
      const float* p = lds + L_RED + (r < 2 ? 128 * aw + 8 * c + 4 * r : 512 + 16 * aw + (c & 12));
      dm = (*reinterpret_cast<const v4f*>(p) + *reinterpret_cast<const v4f*>(p + 576) + *reinterpret_cast<const v4f*>(p + 1152)) * (1.f - gate);
    }
    unsigned t0 = ldsu[L_MISC + 4] >> 24, t1 = ldsu[L_MISC + 5], t2 = ldsu[L_MISC + 7] >> 24, t3 = t1 >> 8;
    unsigned om = r < 2 ? WS_INBOX + ((b * 36 + 8 * aw + (c >> 1)) * 32 + h) * 64 + r * 32 + (c & 1) * 16
                : (lane & 19) ? OOB : WS_INBOX + ((b * 36 + 32 + aw) * 32 + h) * 64 + (c & 4) * 8 + (c & 8) * 2;
    unsigned mo = (L_MRG + 2 * rl) * 4;
    asm volatile("" : "+v"(t0), "+v"(t1), "+v"(t2), "+v"(t3), "+v"(om), "+v"(dm), "+v"(gate), "+v"(mo));
    {
      lds_u32* ctl = (lds_u32*)(ldsu + L_CTL);
      const unsigned tg = ldsu[L_TAG] | 1u;
      const int g = bh >> 4, gr = (int)(__builtin_bitreverse32(g) >> 28), lo = (bh & 15) ^ (gr & 7);
      const int j = lo == 1 ? J1 + gr : lo == 10 ? J2 + gr : g >= 9 && lo == 4 ? J0 + 2 * g - 18 : g >= 9 && lo == 13 ? J0 + 2 * g - 17 : g >= 14 && lo == 7 ? J0 + g : -1;
      if (aw == 2 && j >= 0) {
        lds_wait(ctl, MK0 + MKS * j);
        __builtin_amdgcn_raw_buffer_store_b128(v4u{tg, tg, tg, tg}, rs, lane < 16 ? WS_MARK + ((lane >> 1) * 64 + j) * 32 + (lane & 1) * 16 : OOB, 0, SC1);
      }
      if (aw == 1) {
        unsigned cw = 0u;
#pragma unroll 1
        for (int k = 0; k < 3; ++k) {
          const int j0 = k == 0 ? J0 : k == 1 ? J1 : J2;
          const unsigned at = WS_MARK + (((bh >> 3) & 7) * 64 + j0 + min(lane, NM - 1)) * 32;
          lds_wait(ctl, k == 0 ? RX0 : k == 1 ? RX1 : RX2);
          unsigned v;
          asm volatile("global_load_dword %0, %1, %2 sc1\n\ts_waitcnt vmcnt(0)" : "=v"(v) : "v"(at), "s"(ws) : "memory");
          const int n = j0 + __popcll(__ballot(v == tg && lane < NM));
          const int x = (int)__hip_atomic_load(ctl + r, __ATOMIC_RELAXED, __HIP_MEMORY_SCOPE_WORKGROUP);
          const int late = MK0 + XC + MKS * n - x;
          cw = k == 2 ? (cw & 0xFFFFu) | (unsigned)min(max(FROM - (late * DK2 >> 3), 0), 255) << 16
                      : (unsigned)(x + (max(late, 0) * (k ? DK1 : DK0) >> 3)) | FROM << 16;
          if (c == 0) __hip_atomic_store(ctl + 4 + r, cw, __ATOMIC_RELAXED, __HIP_MEMORY_SCOPE_WORKGROUP);
        }
      }
    }
    lds_wait(cnt + 6, 4u);
    float f[4];
    {
      const v4f a0 = *reinterpret_cast<const v4f*>(lds + L_ML), a1 = *reinterpret_cast<const v4f*>(lds + L_ML + 4);
      const float M = fmaxf(fmaxf(a0[0], a0[2]), fmaxf(a1[0], a1[2]));
      f[0] = __builtin_amdgcn_exp2f(a0[0] - M); f[1] = __builtin_amdgcn_exp2f(a0[2] - M);
      f[2] = __builtin_amdgcn_exp2f(a1[0] - M); f[3] = __builtin_amdgcn_exp2f(a1[2] - M);
      const float inv = 1.f / (f[0] * a0[1] + f[1] * a0[3] + f[2] * a1[1] + f[3] * a1[3]);
#pragma unroll
      for (int w = 0; w < 4; ++w) f[w] *= inv;
      asm volatile("" : "+v"(f[0]), "+v"(f[1]), "+v"(f[2]), "+v"(f[3]));
    }
    wg_barrier();
    {
      const v2f* mrg = reinterpret_cast<const v2f*>(reinterpret_cast<const char*>(lds) + mo);
      const v2f o = (mrg[0] * f[0] + mrg[64] * f[1]) + (mrg[128] * f[2] + mrg[192] * f[3]);
      const float op = __uint_as_float(cvtpk(o[0], o[1]));
      v4f d[9];
#pragma unroll
      for (int j = 0; j < 9; ++j) d[j] = v4f{0.f, 0.f, 0.f, 0.f};
#define WO_M(J, S, O) d[J] = mfma32(O, wv[(J) == 8 ? 32 + (S) : 8 * (S) + (J)], d[J]); SB();
#define WO_B(N, I) N[(I) & 3] = __float_as_uint(dpp<BCAST + (I)>(op)); SB();
#define WO_STEP(S, O, N)                                                                                    \
  WO_M(8, S, O) WO_B(N, 4 * (S) + 4) WO_M(0, S, O) WO_B(N, 4 * (S) + 5) WO_M(1, S, O) WO_B(N, 4 * (S) + 6) WO_M(2, S, O) \
  WO_B(N, 4 * (S) + 7) WO_M(3, S, O) WO_M(4, S, O) WO_M(5, S, O) WO_M(6, S, O) WO_M(7, S, O)
#define WO_SEL(K, J, CTRL, ROW) v[K] = __int_as_float(__builtin_amdgcn_update_dpp(__float_as_int(v[K]), __float_as_int(d[J][0]), CTRL, ROW, 0xf, false)); SB();
      v4u oa, ob;
      SB();
      WO_B(oa, 0) WO_B(oa, 1) WO_B(oa, 2) WO_B(oa, 3)
      WO_STEP(0, oa, ob) WO_STEP(1, ob, oa) WO_STEP(2, oa, ob)
      WO_M(8, 3, ob) WO_M(0, 3, ob) WO_M(1, 3, ob) WO_M(2, 3, ob) WO_M(3, 3, ob) WO_M(4, 3, ob)
      v4f v = {d[0][0], d[1][0], d[2][0], d[3][0]};
      WO_SEL(0, 8, 0x00, 4) WO_SEL(1, 8, 0x55, 4) WO_M(5, 3, ob) WO_SEL(2, 8, 0xAA, 4) WO_M(6, 3, ob) WO_SEL(3, 8, 0xFF, 4) WO_M(7, 3, ob)
      WO_SEL(0, 4, 0xE4, 2) WO_SEL(1, 5, 0xE4, 2) WO_SEL(2, 6, 0xE4, 2) WO_SEL(3, 7, 0xE4, 2)
#undef WO_M
#undef WO_B
#undef WO_STEP
#undef WO_SEL
      v = v * gate + dm;
      __builtin_amdgcn_raw_buffer_store_b128(
          v4u{__builtin_amdgcn_alignbit(t0, __float_as_uint(v[0]), 8), __builtin_amdgcn_alignbit(t1, __float_as_uint(v[1]), 8),
              __builtin_amdgcn_alignbit(t2, __float_as_uint(v[2]), 8), __builtin_amdgcn_alignbit(t3, __float_as_uint(v[3]), 8)}, rs, om, 0, SC1);
    }
  }
}

extern "C" void kernel_launch(void* const* d_in, const int* in_sizes, int n_in, void* d_out, int out_size, void* d_ws,
                              size_t ws_size, hipStream_t stream) {
  (void)in_sizes; (void)n_in; (void)out_size;
  if (ws_size < (size_t)WS_TOTAL) return;
  fused_decode<<<dim3(NB * NH), dim3(512), 0, stream>>>(
      (const char*)d_in[0], (const char*)d_in[1], (const char*)d_in[2], (const char*)d_in[3], (const char*)d_in[4],
      (const char*)d_in[6], (const char*)d_in[7], (const char*)d_in[8], (const char*)d_in[9], (unsigned*)d_out,
      (char*)d_ws);
}
```
